```python
import torch

def _softmax(x, axis=-1):
    return torch.softmax(x, dim=axis)
def _gelu(x):
    return torch.nn.functional.gelu(x, approximate="tanh")
def _silu(x):
    return torch.nn.functional.silu(x)
def _sigmoid(x):
    return torch.sigmoid(x)
def _layernorm(x, axis=-1, eps=1e-5):
    m = x.mean(dim=axis, keepdim=True)
    v = x.var(dim=axis, keepdim=True, unbiased=False)
    return (x - m) / torch.sqrt(v + eps)
def _scatter_add(tgt, idx, src, axis=0):
    return tgt.clone().index_add_(axis, idx, src)
def _segment_sum(src, seg, n):
    out = torch.zeros((n,) + src.shape[1:], dtype=src.dtype)
    return out.index_add_(0, seg, src)
def _pad(x, pw):
    flat = tuple(v for pair in reversed(pw) for v in pair)
    return torch.nn.functional.pad(x, flat)

kernel_name = 'sequential_preLN__spectral_gfnet__mixer_token_mlp__prompt'

def setup_inputs():

    _w = (lambda a: (lambda t: t.to(torch.bfloat16) if t.is_floating_point() else t)(torch.as_tensor(a)))
    def _A_setup():
        B, S, D = 1, 2048, 512
        Df = D // 2 + 1
        return {
            "x": _w((lambda *s: torch.randn(*s, dtype=torch.bfloat16) * 0.05)(B, S, D)),
            "w_r": _w((lambda *s: torch.randn(*s, dtype=torch.bfloat16) * 0.2)(S, Df) * 0.02),
            "w_i": _w((lambda *s: torch.randn(*s, dtype=torch.bfloat16) * 0.2)(S, Df) * 0.02),
        }
    _A_in = _A_setup()

    def _B_setup():
        B, S, D, F = 1, 2048, 512, 1376
        return {
            "x": _w((lambda *s: torch.randn(*s, dtype=torch.bfloat16) * 0.05)(B, S, D)),
            "w1": _w((lambda *s: torch.randn(*s, dtype=torch.bfloat16) * 0.4)(S, F) * (S ** -0.5)),
            "b1": _w((lambda *s: torch.randn(*s, dtype=torch.bfloat16) * 3.2)(F) * 0.02),
            "w2": _w((lambda *s: torch.randn(*s, dtype=torch.bfloat16) * 0.2)(F, S) * (F ** -0.5)),
            "b2": _w((lambda *s: torch.randn(*s, dtype=torch.bfloat16) * 0.4)(S) * 0.02),
        }
    _B_in = _B_setup()

    return {
        "A_x": _A_in["x"],
        "A_w_r": _A_in["w_r"],
        "A_w_i": _A_in["w_i"],
        "B_x": _B_in["x"],
        "B_w1": _B_in["w1"],
        "B_b1": _B_in["b1"],
        "B_w2": _B_in["w2"],
        "B_b2": _B_in["b2"],
    }

def reference(A_x, A_w_r, A_w_i, B_x, B_w1, B_b1, B_w2, B_b2):
    def _A_ref(x, w_r, w_i):
        B, S, D = x.shape
        X = (lambda x, axes=(-2,-1): torch.fft.rfft2(x.float(), dim=axes))(x, axes=(-2, -1))
        W = w_r + 1j * w_i
        X = X * W
        y = (lambda x, s, axes=(-2,-1): torch.fft.irfft2(x, s=s, dim=axes).to(torch.bfloat16))(X, (S, D), axes=(-2, -1))
        return y

    def _B_ref(x, w1, b1, w2, b2):
        B, S, D = x.shape
        h = _layernorm(x, axis=-1)
        u = torch.einsum("bsd,sf->bdf", h, w1) + b1
        u = _gelu(u)
        y = torch.einsum("bdf,fs->bds", u, w2) + b2
        return (lambda x, *d: x.permute(*d))(y, 0, 2, 1)

    x = A_x
    h = x + _A_ref(_layernorm(x, axis=-1), A_w_r, A_w_i)
    h2 = h + _B_ref(_layernorm(h, axis=-1), B_w1, B_b1, B_w2, B_b2)
    return h2
```

```cpp
#include <hip/hip_runtime.h>
#include <hip/hip_bf16.h>

typedef _Float16 h8 __attribute__((ext_vector_type(8)));
typedef _Float16 h2 __attribute__((ext_vector_type(2)));
typedef __bf16 b8 __attribute__((ext_vector_type(8)));
typedef __bf16 b2 __attribute__((ext_vector_type(2)));
typedef float f2 __attribute__((ext_vector_type(2)));
typedef float f4 __attribute__((ext_vector_type(4)));
typedef short s4 __attribute__((ext_vector_type(4)));
typedef unsigned u2 __attribute__((ext_vector_type(2)));
typedef unsigned u4 __attribute__((ext_vector_type(4)));
#define LAS __attribute__((address_space(3)))
#define DEV __device__ __forceinline__
#define FENCE() asm volatile("" ::: "memory")

#define STAMPS 0
#define PRE1 20
#define PRE2 20
#define PRE3 20
#define PRE4 20
#define WINL 9

constexpr int SC1 = 16;
constexpr unsigned SENT = 0xFFFFFFFFu;
constexpr unsigned SPIN_CAP = 1u << 14;
constexpr unsigned WS_X1 = 0, WS_X2 = 2u << 20, WS_HT = 4u << 20, WS_H = 6u << 20, WS_G = 8u << 20;
constexpr unsigned G_BYTES = 8u * 172u * 1024u;
constexpr unsigned WS_END = WS_G + G_BYTES;
constexpr unsigned WS_STAMP = 16u << 20;
constexpr unsigned L_W = 0, L_WREG = 36864, L_A = 4 * L_WREG, L_FLAG = L_A + 8192, L_BYTES = L_FLAG + 64;

DEV f4 mfma_h(u4 a, u4 b, f4 c) { return __builtin_amdgcn_mfma_f32_16x16x32_f16(__builtin_bit_cast(h8, a), __builtin_bit_cast(h8, b), c, 0, 0, 0); }
DEV f4 mfma_b(u4 a, u4 b, f4 c) { return __builtin_amdgcn_mfma_f32_16x16x32_bf16(__builtin_bit_cast(b8, a), __builtin_bit_cast(b8, b), c, 0, 0, 0); }
DEV unsigned pkh(float a, float b) { f2 v = {a, b}; return __builtin_bit_cast(unsigned, __builtin_convertvector(v, h2)); }
DEV unsigned pkb(float a, float b) { f2 v = {a, b}; return __builtin_bit_cast(unsigned, __builtin_convertvector(v, b2)); }
DEV float blo(unsigned u) { return __builtin_bit_cast(float, u << 16); }
DEV float bhi(unsigned u) { return __builtin_bit_cast(float, u & 0xFFFF0000u); }
DEV float cosr(float rev) { return __builtin_amdgcn_cosf(rev); }
DEV float sinr(float rev) { return __builtin_amdgcn_sinf(rev); }
DEV u2 trrd(LAS unsigned char* p) { return __builtin_bit_cast(u2, __builtin_amdgcn_ds_read_tr16_b64_v4i16((LAS s4*)p)); }
DEV u4 cat(u2 a, u2 b) { u4 r = {a.x, a.y, b.x, b.y}; return r; }
DEV u4 neg(u4 a) { return a ^ 0x80008000u; }
DEV u4 clampS(u4 v) { u4 r = {min(v.x, SENT - 1), min(v.y, SENT - 1), min(v.z, SENT - 1), min(v.w, SENT - 1)}; return r; }
DEV unsigned mx(unsigned m, u4 v) { return max(max(max(m, v.x), v.y), max(v.z, v.w)); }
DEV u4 ldx(__amdgpu_buffer_rsrc_t r, unsigned v, unsigned s) { return __builtin_amdgcn_raw_buffer_load_b128(r, v, s, SC1); }
DEV void stx(u4 d, __amdgpu_buffer_rsrc_t r, unsigned v, unsigned s) { __builtin_amdgcn_raw_buffer_store_b128(d, r, v, s, SC1); }
DEV bool bad4(const u4 (&v)[4]) { return __builtin_amdgcn_ballot_w64(mx(mx(mx(mx(0u, v[0]), v[1]), v[2]), v[3]) == SENT) != 0; }
DEV void reload4(u4 (&v)[4], __amdgpu_buffer_rsrc_t r, unsigned vo, unsigned so) {
#pragma unroll 1
  for (unsigned s = 0; s < SPIN_CAP; ++s) {
    FENCE();
#pragma unroll
    for (int mt = 0; mt < 4; ++mt) v[mt] = ldx(r, vo + mt * 256, so);
    if (!bad4(v)) break;
  }
}
template <int C> DEV float ror_add(float v) {
  return v + __builtin_bit_cast(float, __builtin_amdgcn_update_dpp(0, __builtin_bit_cast(int, v), C, 0xF, 0xF, false));
}
DEV f2 swap32(float a, float b) { auto r = __builtin_amdgcn_permlane32_swap(__builtin_bit_cast(unsigned, a), __builtin_bit_cast(unsigned, b), false, false); const unsigned r0 = r[0], r1 = r[1]; f2 o = {__builtin_bit_cast(float, r0), __builtin_bit_cast(float, r1)}; return o; }
DEV f2 swap16(float a, float b) { auto r = __builtin_amdgcn_permlane16_swap(__builtin_bit_cast(unsigned, a), __builtin_bit_cast(unsigned, b), false, false); const unsigned r0 = r[0], r1 = r[1]; f2 o = {__builtin_bit_cast(float, r0), __builtin_bit_cast(float, r1)}; return o; }

DEV void row_stats(const float (&v0)[8], const float (&v1)[8], float& mean, float& var) {
  float s0 = 0.f, q0 = 0.f, s1 = 0.f, q1 = 0.f;
#pragma unroll
  for (int j = 0; j < 8; ++j) { s0 += v0[j]; q0 = fmaf(v0[j], v0[j], q0); s1 += v1[j]; q1 = fmaf(v1[j], v1[j], q1); }
  f2 a = swap32(s0, s1), b = swap32(q0, q1);
  f2 c = swap16(a.x + a.y, b.x + b.y);
  float t = c.x + c.y;
  t = ror_add<0x128>(t); t = ror_add<0x124>(t); t = ror_add<0x122>(t); t = ror_add<0x121>(t);
  f2 d = swap16(t, t);
  mean = d.x * (1.f / 512.f);
  var = fmaxf(fmaf(-mean, mean, d.y * (1.f / 512.f)), 0.f);
}

DEV void sig(LAS unsigned char* L, int slot, unsigned e) { FENCE(); *(volatile LAS unsigned*)(L + L_FLAG + 4 * slot) = e; FENCE(); }
DEV void wait4(LAS unsigned char* L, unsigned e) {
  for (;;) { FENCE(); u4 f = *(volatile LAS u4*)(L + L_FLAG); FENCE();
    if (__builtin_amdgcn_readfirstlane(min(min(f.x, f.y), min(f.z, f.w))) >= e) break; }
}
DEV void wait1(LAS unsigned char* L, int slot, unsigned e) {
  for (;;) { FENCE(); unsigned f = *(volatile LAS unsigned*)(L + L_FLAG + 4 * slot); FENCE();
    if (__builtin_amdgcn_readfirstlane(f) >= e) break; __builtin_amdgcn_s_sleep(1); }
}
DEV void dma16(__amdgpu_buffer_rsrc_t r, LAS unsigned char* dst, unsigned v, unsigned s) {
  __builtin_amdgcn_raw_ptr_buffer_load_lds(r, (LAS void*)dst, 16, v, s, 0, 0);
}
#define STAMP(i)

DEV void cmul(float& ar, float& ai, float br, float bi) { float r = ar * br - ai * bi; ai = ar * bi + ai * br; ar = r; }

__global__ void __launch_bounds__(320, 2)
fused_kernel(const unsigned short* __restrict__ x, const unsigned short* __restrict__ w_r, const unsigned short* __restrict__ w_i,
             const unsigned short* __restrict__ w1, const unsigned short* __restrict__ b1, const unsigned short* __restrict__ w2,
             const unsigned short* __restrict__ b2, unsigned short* __restrict__ out, unsigned char* ws) {
  extern __shared__ __attribute__((aligned(16))) unsigned char smem[];
  LAS unsigned char* L = (LAS unsigned char*)smem;
  const int tid = threadIdx.x, lane = tid & 63, wv = __builtin_amdgcn_readfirstlane(tid >> 6);
  const int g = lane >> 4, c = lane & 15;
  const int xcd = blockIdx.x & 7, i32 = blockIdx.x >> 3, b = xcd * 32 + i32;
  const int s0 = (b & 0xC0) | ((b & 3) << 4) | ((b >> 2) & 15);
  const int dt = 4 * (xcd & 1) + (i32 & 3), fq = xcd >> 1;
  const int ft = (fq ? 1 + 7 * fq : 0) + (i32 >> 2);
  const bool g1 = (i32 >> 2) < (fq ? 7 : 8);
  const int f0 = min(48 * ft, 1328);
  const int st = 8 * fq + (i32 >> 2);
  STAMP(0);
  const __amdgpu_buffer_rsrc_t rws = __builtin_amdgcn_make_buffer_rsrc((void*)ws, 0, WS_END, 0x00020000);

  if (wv == 4) {
    if (lane < 8) *(volatile LAS unsigned*)(L + L_FLAG + 4 * lane) = 0u;
    __syncthreads();
    const __amdgpu_buffer_rsrc_t r1 = __builtin_amdgcn_make_buffer_rsrc((void*)w1, 0, 2048 * 1376 * 2, 0x00020000);
    const __amdgpu_buffer_rsrc_t r2 = __builtin_amdgcn_make_buffer_rsrc((void*)w2, 0, 2048 * 1376 * 2, 0x00020000);
    if (g1) {
      unsigned vo[3];
#pragma unroll
      for (int n = 0; n < 3; ++n) {
        const int pc = 64 * n + lane, rho = pc / 6, pcol = pc - 6 * rho;
        const int gg = (rho >> 2) & 3, r = 4 * (rho >> 4) + (rho & 3);
        vo[n] = (16 * gg + 256 * r) * 2752 + f0 * 2 + pcol * 16;
      }
      for (int ks = 0; ks < 16; ++ks) {
        if (ks == 12) wait4(L, 5);
        const int slot = ks < 12 ? ks : ks - 12;
#pragma unroll
        for (int v = 0; v < 4; ++v) {
#pragma unroll
          for (int n = 0; n < 3; ++n) {
            dma16(r1, L + L_W + v * L_WREG + slot * 3072 + n * 1024, vo[n], (64 * v + ks) * 2752);
            asm volatile("s_waitcnt vmcnt(%0)" ::"i"(WINL) : "memory");
          }
        }
        sig(L, 4, ks);
      }
      asm volatile("s_waitcnt vmcnt(0)" ::: "memory");
    }
    sig(L, 4, 16);
    unsigned vo2[4];
#pragma unroll
    for (int n = 0; n < 4; ++n) {
      const int rho = 8 * n + (lane >> 3), pp = lane & 7;
      const int fr = 8 * ((rho >> 2) & 3) + 4 * (rho >> 4) + (rho & 3);
      vo2[n] = fr * 4096 + st * 128 + ((pp ^ (((rho >> 1) & 3) << 1)) * 16);
    }
    wait4(L, 9);
    for (int ks = 0; ks < 11; ++ks) {
      if (ks == 6) { asm volatile("s_waitcnt vmcnt(32)" ::: "memory"); sig(L, 4, 20); wait4(L, 11); }
      if (ks == 9) wait4(L, 12);
      const int so = ks < 6 ? 12288 + ks * 4096 : (ks < 9 ? (ks - 6) * 4096 : 12288 + (ks - 9) * 4096);
#pragma unroll
      for (int v = 0; v < 4; ++v) {
#pragma unroll
        for (int n = 0; n < 4; ++n) dma16(r2, L + L_W + v * L_WREG + so + n * 1024, vo2[n], (11 * v + ks) * (32 * 4096));
      }
      asm volatile("s_waitcnt vmcnt(32)" ::: "memory");
    }
    sig(L, 4, 25);
    asm volatile("s_waitcnt vmcnt(0)" ::: "memory");
    sig(L, 4, 27);
    return;
  }

  const int chunk = (lane & 32) | ((lane & 15) << 1) | ((lane >> 4) & 1);
  const u4 xa = *(const u4*)(x + (size_t)(s0 + 512 * wv) * 512 + 8 * chunk);
  const u4 xb = *(const u4*)(x + (size_t)(s0 + 512 * wv + 256) * 512 + 8 * chunk);
  float wre[2][4], wim[2][4];
  {
    const int pw = b >> 6, pt = (b >> 5) & 1, p = pt ? (pw ? 8 - pw : 4) : pw, k2 = (b >> 1) & 15;
    const __amdgpu_buffer_rsrc_t rr = __builtin_amdgcn_make_buffer_rsrc((void*)w_r, 0, 2048 * 257 * 2, 0x00020000);
    const __amdgpu_buffer_rsrc_t ri = __builtin_amdgcn_make_buffer_rsrc((void*)w_i, 0, 2048 * 257 * 2, 0x00020000);
    const int kB = 8 * (b & 1) + 2 * wv + 1 + 32 * k2;
    const bool mir = k2 >= 8;
    const int col = mir ? 512 - kB : kB - 1;
    unsigned dr[4], di[4];
#pragma unroll
    for (int i = 0; i < 4; ++i) {
      const int j = p + 8 * (c + 16 * (4 * g + i)), jj = mir ? ((2048 - j) & 2047) : j;
      const unsigned o = (jj * 257 + col) * 2;
      dr[i] = __builtin_amdgcn_raw_buffer_load_b32(rr, o, 0, 0);
      di[i] = __builtin_amdgcn_raw_buffer_load_b32(ri, o, 0, 0);
    }
#pragma unroll
    for (int i = 0; i < 4; ++i) {
      wre[0][i] = mir ? bhi(dr[i]) : blo(dr[i]); wre[1][i] = mir ? blo(dr[i]) : bhi(dr[i]);
      wim[0][i] = mir ? -bhi(di[i]) : blo(di[i]); wim[1][i] = mir ? -blo(di[i]) : bhi(di[i]);
    }
    if ((b & 1) == 0 && wv == 0) {
      const int k = pw == 0 ? 16 * (k2 ? k2 : 16) : 32 * k2 + 16 * pt;
      const float fa = (k & 255) == 0 ? 0.5f : (k < 256 ? 1.f : 0.f), fb = 1.f - fa;
      const int ca = min(k, 256), cb = min((512 - k) & 511, 256);
#pragma unroll
      for (int i = 0; i < 4; ++i) {
        const int j = p + 8 * (c + 16 * (4 * g + i)), jm = (2048 - j) & 2047;
        const float ar = blo(w_r[j * 257 + ca]), ai = blo(w_i[j * 257 + ca]), br = blo(w_r[jm * 257 + cb]), bi = blo(w_i[jm * 257 + cb]);
        wre[0][i] = fb == 0.f ? ar : (fa == 0.f ? br : 0.5f * (ar + br));
        wim[0][i] = fb == 0.f ? ai : (fa == 0.f ? -bi : 0.5f * (ai - bi));
      }
    }
  }
  __syncthreads();
  const int pA = wv, pB = wv ? 8 - wv : 4;
  float c16[4], s16[4];
#pragma unroll
  for (int i = 0; i < 4; ++i) { const float ph = (float)(((4 * g + i) * c) & 15) * (1.f / 16.f); c16[i] = cosr(ph); s16[i] = sinr(ph); }
  u4 FR, FI;
#pragma unroll
  for (int i = 0; i < 4; ++i) { FR[i] = pkh(0.25f * c16[i], 0.25f * s16[i]); FI[i] = pkh(-0.25f * s16[i], 0.25f * c16[i]); }
  const f4 Z = {0.f, 0.f, 0.f, 0.f};

  {
    u4 AC;
    {
      const int i = c & 3; const bool on = (c >> 2) == g && i < 2;
      const int mult = wv ? wv : 4 * i; const float sh = wv ? 0.25f * i : 0.f;
#pragma unroll
      for (int j = 0; j < 4; ++j) { const unsigned v = pkh(0.5f * cosr((float)((2 * j * mult) & 7) * 0.125f - sh), 0.5f * cosr((float)(((2 * j + 1) * mult) & 7) * 0.125f - sh)); AC[j] = on ? v : 0u; }
    }
    u4 C0, S0;
#pragma unroll
    for (int j = 0; j < 4; ++j) {
      const float p0 = (float)(((8 * j + g) * c) & 31) * (1.f / 32.f), p1 = (float)(((8 * j + 4 + g) * c) & 31) * (1.f / 32.f);
      C0[j] = pkh(0.125f * cosr(p0), 0.125f * cosr(p1)); S0[j] = pkh(0.125f * sinr(p0), 0.125f * sinr(p1));
    }
    const unsigned altv = (g & 1) ? 0xB000B000u : 0x30003000u;
    const u4 ALT = {altv, altv, altv, altv}, ZU = {0u, 0u, 0u, 0u};
    const u4 NS = neg(S0), C1 = c ? C0 : ALT, NSA = c ? NS : ALT;
    const u4 Mare = C0, Maro = wv ? NS : ZU, Maie = wv ? NS : NSA, Maio = wv ? neg(C0) : ZU;
    const u4 Mbre = wv ? C1 : ZU, Mbro = wv ? S0 : C0, Mbie = wv ? NS : ZU, Mbio = wv ? C1 : NSA;
    float tc[2][4], ts[2][4];
#pragma unroll
    for (int t = 0; t < 2; ++t)
#pragma unroll
      for (int i = 0; i < 4; ++i) {
        const int kk = (t && c == 0) ? 16 : c;
        int n = (4 * (4 * g + i) * kk + s0 * (t ? pB : pA)) & 2047;
        if (wv == 0 && c == 0) n = 0;
        const float ph = (float)n * (1.f / 2048.f);
        tc[t][i] = cosr(ph); ts[t][i] = -sinr(ph);
      }
    u4 GR[2] = {}, GI[2] = {};
    if (wv == 0) {
      const int mm = c ? c : 16;
#pragma unroll
      for (int t = 0; t < 2; ++t)
#pragma unroll
        for (int i = 0; i < 4; ++i) {
          const float ph = (float)((64 * mm * (4 * g + i) + s0 * 4 * t) & 2047) * (1.f / 2048.f);
          const float cc = 0.25f * cosr(ph), ss = -0.25f * sinr(ph);
          GR[t][i] = (mm & 1) ? pkh(0.f, cc) : pkh(cc, 0.f); GI[t][i] = (mm & 1) ? pkh(0.f, ss) : pkh(ss, 0.f);
        }
    }
    float v0[8], v1[8];
#pragma unroll
    for (int j = 0; j < 4; ++j) { v0[2 * j] = blo(xa[j]); v0[2 * j + 1] = bhi(xa[j]); v1[2 * j] = blo(xb[j]); v1[2 * j + 1] = bhi(xb[j]); }
    STAMP(1);
    float mean, var; row_stats(v0, v1, mean, var);
    const float rs = __builtin_amdgcn_rsqf(var + 1e-5f);
    const f2 r2 = swap32(rs, rs), m2 = swap32(-mean * rs, -mean * rs);
    u4 na, nb;
#pragma unroll
    for (int j = 0; j < 4; ++j) { na[j] = pkh(fmaf(v0[2 * j], r2.x, m2.x), fmaf(v0[2 * j + 1], r2.x, m2.x)); nb[j] = pkh(fmaf(v1[2 * j], r2.y, m2.y), fmaf(v1[2 * j + 1], r2.y, m2.y)); }
    *(LAS u4*)(L + L_A + (2 * wv) * 1024 + ((16 * chunk) ^ (((2 * wv) & 3) << 6))) = na;
    *(LAS u4*)(L + L_A + (2 * wv + 1) * 1024 + ((16 * chunk) ^ (((2 * wv + 1) & 3) << 6))) = nb;
    sig(L, wv, 1); wait4(L, 1);
    float e[8], o[8];
    {
      const int q = (lane >> 2) & 3, p = lane & 3;
#pragma unroll
      for (int j = 0; j < 8; ++j) {
        const unsigned off = L_A + q * 1024 + ((128 * j + 32 * g + 8 * p) ^ (q << 6));
        const u4 bf = cat(trrd(L + off), trrd(L + off + 4096));
        const f4 d = mfma_h(AC, bf, Z); e[j] = d[0]; o[j] = d[1];
      }
    }
    sig(L, wv, 2);
    u4 E, O;
#pragma unroll
    for (int j = 0; j < 4; ++j) { E[j] = pkh(e[2 * j], e[2 * j + 1]); O[j] = pkh(o[2 * j], o[2 * j + 1]); }
    f4 zr[2], zi[2];
    zr[0] = mfma_h(O, Maro, mfma_h(E, Mare, Z)); zi[0] = mfma_h(O, Maio, mfma_h(E, Maie, Z));
    zr[1] = mfma_h(O, Mbro, mfma_h(E, Mbre, Z)); zi[1] = mfma_h(O, Mbio, mfma_h(E, Mbie, Z));
#pragma unroll
    for (int t = 0; t < 2; ++t) {
      u4 zp;
#pragma unroll
      for (int i = 0; i < 4; ++i) { float a = zr[t][i], bb = zi[t][i]; cmul(a, bb, tc[t][i], ts[t][i]); zp[i] = pkh(a, bb); }
      f4 yr = mfma_h(zp, FR, Z), yi = mfma_h(zp, FI, Z);
      { const f4 gr = mfma_h(zp, GR[t], Z), gi = mfma_h(zp, GI[t], Z); yr[0] = tid >= 16 ? yr[0] : gr[0]; yi[0] = tid >= 16 ? yi[0] : gi[0]; }
      u4 ov;
#pragma unroll
      for (int i = 0; i < 4; ++i) ov[i] = pkh(yr[i], yi[i]);
      stx(clampS(ov), rws, (c * 4 + g) * 16, WS_X1 + b * 8192 + (wv * 2 + t) * 1024);
    }
  }
  STAMP(2);
  u4 IR, II;
#pragma unroll
  for (int i = 0; i < 4; ++i) { IR[i] = pkb(c16[i], -s16[i]); II[i] = pkb(s16[i], c16[i]); }
  {
    float tc[4], ts[4];
#pragma unroll
    for (int i = 0; i < 4; ++i) { const float ph = (float)((4 * g + i) * c) * (1.f / 256.f); tc[i] = cosr(ph); ts[i] = -sinr(ph); }
    __builtin_amdgcn_s_sleep(PRE1);
    wait4(L, 2);
    u4 v0, v1;
#pragma unroll 1
    for (unsigned s = 0; s < SPIN_CAP; ++s) {
      FENCE();
      v0 = ldx(rws, (tid >> 1) * 8192 + (tid & 1) * 16, WS_X1 + b * 32);
      v1 = ldx(rws, (tid >> 1) * 8192 + (tid & 1) * 16, WS_X1 + b * 32 + 128 * 8192);
      if (!__builtin_amdgcn_ballot_w64(mx(mx(0u, v0), v1) == SENT)) break;
    }
    STAMP(3);
    {
      LAS unsigned char* pw = L + L_A + (tid & 1) * 4096 + (((tid >> 1) * 4) ^ ((tid & 1) << 6));
#pragma unroll
      for (int i = 0; i < 4; ++i) { *(LAS unsigned*)(pw + i * 1024) = v0[i]; *(LAS unsigned*)(pw + i * 1024 + 512) = v1[i]; }
    }
    sig(L, wv, 3); wait4(L, 3);
    f4 orr[2], oi[2];
#pragma unroll
    for (int t = 0; t < 2; ++t) {
      const u4 a = *(LAS u4*)(L + L_A + (2 * wv + t) * 1024 + ((16 * lane) ^ ((wv >> 1) << 6)));
      f4 r = mfma_h(a, FR, Z), im = mfma_h(a, FI, Z);
      u4 z;
#pragma unroll
      for (int i = 0; i < 4; ++i) { float u = r[i], v = im[i]; cmul(u, v, tc[i], ts[i]); z[i] = pkh(u, v); }
      r = mfma_h(FR, z, Z); im = mfma_h(FI, z, Z);
#pragma unroll
      for (int i = 0; i < 4; ++i) { float u = r[i], v = im[i]; cmul(u, v, wre[t][i], wim[t][i]); z[i] = pkb(u, v); }
      r = mfma_b(z, IR, Z); im = mfma_b(z, II, Z);
#pragma unroll
      for (int i = 0; i < 4; ++i) { float u = r[i], v = im[i]; cmul(u, v, tc[i], -ts[i]); z[i] = pkb(u, v); }
      orr[t] = mfma_b(IR, z, Z); oi[t] = mfma_b(II, z, Z);
    }
    u4 pr, pi;
#pragma unroll
    for (int i = 0; i < 4; ++i) { pr[i] = pkb(orr[0][i], orr[1][i]); pi[i] = pkb(oi[0][i], oi[1][i]); }
    FENCE();
    *(LAS u4*)(L + L_A + (2 * wv) * 1024 + 16 * lane) = pr;
    *(LAS u4*)(L + L_A + (2 * wv + 1) * 1024 + ((16 * lane) ^ 64)) = pi;
    sig(L, wv, 4); wait4(L, 4);
    LAS unsigned char* prd = L + L_A + (tid & 1) * 1024 + (((tid >> 1) * 4) ^ ((tid & 1) << 6));
#pragma unroll
    for (int u = 0; u < 2; ++u) {
      u4 ov;
#pragma unroll
      for (int v = 0; v < 4; ++v) ov[v] = *(LAS unsigned*)(prd + v * 2048 + u * 512);
      stx(clampS(ov), rws, tid * 16, WS_X2 + b * 8192 + u * 4096);
    }
  }
  STAMP(4);
  u4 WA[4][3];
  const unsigned a1off = L_W + wv * L_WREG + (4 * g + ((lane >> 2) & 3)) * 96;
  if (g1) {
    wait1(L, 4, 4);
#pragma unroll
    for (int kk = 0; kk < 4; ++kk)
#pragma unroll
      for (int nt = 0; nt < 3; ++nt) {
        const unsigned off = a1off + kk * 3072 + (nt < 2 ? 16 * (lane & 3) + 8 * nt : 64 + 8 * (lane & 3));
        WA[kk][nt] = cat(trrd(L + off), trrd(L + off + 1536));
      }
  }
  sig(L, wv, 5);
  {
    u4 PR, PI;
    PR[0] = pkb(c16[0], c16[1]); PR[1] = pkb(c16[2], c16[3]); PI[0] = pkb(s16[0], s16[1]); PI[1] = pkb(s16[2], s16[3]);
    PR[2] = PI[0] ^ 0x80008000u; PR[3] = PI[1] ^ 0x80008000u; PI[2] = PR[0]; PI[3] = PR[1];
    u4 GR[2] = {}, GI[2] = {};
    if (wv == 0) {
#pragma unroll
      for (int t = 0; t < 2; ++t) {
        float cc[4], ss[4];
#pragma unroll
        for (int i = 0; i < 4; ++i) {
          const int m = 4 * g + i, mm = m ? m : 16;
          const float ph = (float)((64 * mm * c + s0 * 4 * t) & 2047) * (1.f / 2048.f), wg = m ? 1.f : 0.5f;
          cc[i] = wg * cosr(ph); ss[i] = -wg * sinr(ph);
        }
        GR[t][0] = pkb(cc[0], 0.f); GR[t][1] = pkb(cc[2], 0.f); GR[t][2] = pkb(ss[0], 0.f); GR[t][3] = pkb(ss[2], 0.f);
        GI[t][0] = pkb(0.f, cc[1]); GI[t][1] = pkb(0.f, cc[3]); GI[t][2] = pkb(0.f, ss[1]); GI[t][3] = pkb(0.f, ss[3]);
      }
    }
    float tc[2][4], ts[2][4];
#pragma unroll
    for (int t = 0; t < 2; ++t)
#pragma unroll
      for (int i = 0; i < 4; ++i) {
        const int k1 = 4 * g + i, kk = (t && k1 == 0) ? 16 : k1;
        int n = (4 * c * kk + s0 * (t ? pB : pA)) & 2047;
        if (wv == 0 && k1 == 0) n = 0;
        const float ph = (float)n * (1.f / 2048.f);
        tc[t][i] = cosr(ph); ts[t][i] = sinr(ph);
      }
    u4 Mea, Meb, Moa, Mob;
#pragma unroll
    for (int i = 0; i < 4; ++i) {
      const int k1 = 4 * g + i;
      const float ph = (float)((c * k1) & 31) * (1.f / 32.f), cs = cosr(ph), sn = sinr(ph), alt = (c & 1) ? -1.f : 1.f;
      const float c1 = k1 ? cs : alt, nsa = k1 ? -sn : alt;
      Mea[i] = pkb(cs, wv ? -sn : nsa); Meb[i] = wv ? pkb(c1, -sn) : 0u;
      Moa[i] = wv ? pkb(-sn, -cs) : 0u; Mob[i] = wv ? pkb(sn, c1) : pkb(cs, nsa);
    }
    const u4 SG = {0u, 0x80008000u, 0u, 0x80008000u};
    u4 AI;
    {
      const int i = c & 3, r = 2 * wv + i; const bool on = (c >> 2) == g && i < 2;
      const float sc = 1.f / 512.f;
#pragma unroll
      for (int j = 0; j < 4; ++j) {
        const float ph = (float)((r * j) & 7) * 0.125f;
        const unsigned v = j ? pkb(sc * cosr(ph), sc * sinr(ph)) : pkb(sc, (r & 1) ? -sc : sc);
        AI[j] = on ? v : 0u;
      }
    }
    __builtin_amdgcn_s_sleep(PRE2);
    wait4(L, 5);
    u4 v0, v1;
#pragma unroll 1
    for (unsigned s = 0; s < SPIN_CAP; ++s) {
      FENCE();
      v0 = ldx(rws, (lane >> 1) * 8192 + (lane & 1) * 16, WS_X2 + b * 32 + wv * 64 * 8192);
      v1 = ldx(rws, (lane >> 1) * 8192 + (lane & 1) * 16, WS_X2 + b * 32 + (wv * 64 + 32) * 8192);
      if (!__builtin_amdgcn_ballot_w64(mx(mx(0u, v0), v1) == SENT)) break;
    }
    STAMP(5);
    LAS unsigned char* pv = L + L_A + wv * 2048;
    *(LAS u4*)(pv + (lane & 1) * 512 + (((lane >> 1) * 16) ^ ((lane & 1) << 6))) = v0;
    *(LAS u4*)(pv + 1024 + (lane & 1) * 512 + (((lane >> 1) * 16) ^ ((lane & 1) << 6))) = v1;
    FENCE();
    u4 U[2];
#pragma unroll
    for (int t = 0; t < 2; ++t) {
      const u4 z = cat(trrd(pv + t * 1024 + 8 * lane), trrd(pv + t * 1024 + 512 + ((8 * lane) ^ 64)));
      f4 r = mfma_b(z, PR, Z), im = mfma_b(z, PI, Z);
      { const f4 gr = mfma_b(z, GR[t], Z), gi = mfma_b(z, GI[t], Z); r[0] = tid >= 16 ? r[0] : gr[0]; im[0] = tid >= 16 ? im[0] : gi[0]; }
#pragma unroll
      for (int i = 0; i < 4; ++i) { float u = r[i], v = im[i]; cmul(u, v, tc[t][i], ts[t][i]); U[t][i] = pkb(u, v); }
    }
    FENCE();
    {
      const int jj = c & 7, fj = (jj & 3) | ((jj >> 2) << 3);
#pragma unroll
      for (int ta = 0; ta < 2; ++ta) {
        const f4 ev = mfma_b(ta ? Meb ^ SG : Meb, U[1], mfma_b(ta ? Mea ^ SG : Mea, U[0], Z));
        const f4 ov = mfma_b(ta ? Mob ^ SG : Mob, U[1], mfma_b(ta ? Moa ^ SG : Moa, U[0], Z));
        const unsigned o = 128 * jj + 8 * ((8 * ta + 4 * (c >> 3) + g) ^ fj);
        const u2 pe = {pkb(ev[0], ev[1]), pkb(ev[2], ev[3])}, po = {pkb(ov[0], ov[1]), pkb(ov[2], ov[3])};
        *(LAS u2*)(pv + (o ^ (((2 * wv) & 3) << 6))) = pe;
        *(LAS u2*)(pv + 1024 + (o ^ (((2 * wv + 1) & 3) << 6))) = po;
      }
    }
    sig(L, wv, 6); wait4(L, 6);
    float h0[8], h1[8];
    {
      const int q = (lane >> 2) & 3, p = lane & 3;
#pragma unroll
      for (int j = 0; j < 8; ++j) {
        const int fj = (j & 3) | ((j >> 2) << 3);
        const unsigned off = L_A + q * 1024 + ((128 * j + 8 * ((4 * g + p) ^ fj)) ^ (q << 6));
        const u4 bf = cat(trrd(L + off), trrd(L + off + 4096));
        const f4 xi = {(j & 1) ? bhi(xa[j >> 1]) : blo(xa[j >> 1]), (j & 1) ? bhi(xb[j >> 1]) : blo(xb[j >> 1]), 0.f, 0.f};
        const f4 d = mfma_b(AI, bf, xi); h0[j] = d[0]; h1[j] = d[1];
      }
    }
    sig(L, wv, 7);
    float mean, var; row_stats(h0, h1, mean, var);
    const float i1 = __builtin_amdgcn_rsqf(var + 1e-5f);
    const float rs = i1 * __builtin_amdgcn_rsqf(fmaf(var * i1, i1, 1e-5f));
    const f2 r2 = swap32(rs, rs), m2 = swap32(-mean * rs, -mean * rs);
    u4 na, nb, ha, hb;
#pragma unroll
    for (int j = 0; j < 4; ++j) {
      na[j] = pkb(fmaf(h0[2 * j], r2.x, m2.x), fmaf(h0[2 * j + 1], r2.x, m2.x)); nb[j] = pkb(fmaf(h1[2 * j], r2.y, m2.y), fmaf(h1[2 * j + 1], r2.y, m2.y));
      ha[j] = pkb(h0[2 * j], h0[2 * j + 1]); hb[j] = pkb(h1[2 * j], h1[2 * j + 1]);
    }
    wait4(L, 7);
    *(LAS u4*)(L + L_A + (2 * wv) * 1024 + ((16 * chunk) ^ (((2 * wv) & 3) << 6))) = na;
    *(LAS u4*)(L + L_A + (2 * wv + 1) * 1024 + ((16 * chunk) ^ (((2 * wv + 1) & 3) << 6))) = nb;
    sig(L, wv, 8); wait4(L, 8);
    {
      const int q = (lane >> 2) & 3, p = lane & 3;
#pragma unroll
      for (int u = 0; u < 2; ++u) {
        const unsigned off = L_A + q * 1024 + ((128 * (2 * wv + u) + 64 * (g >> 1) + 16 * p + 8 * (g & 1)) ^ (q << 6));
        stx(clampS(cat(trrd(L + off), trrd(L + off + 4096))), rws, lane * 16, WS_HT + (2 * wv + u) * 262144 + b * 1024);
      }
    }
    stx(clampS(ha), rws, chunk * 16, WS_H + (s0 + 512 * wv) * 1024);
    stx(clampS(hb), rws, chunk * 16, WS_H + (s0 + 512 * wv + 256) * 1024);
  }
  STAMP(6);
  if (g1) {
    float bias[3][4];
#pragma unroll
    for (int nt = 0; nt < 3; ++nt)
#pragma unroll
      for (int i = 0; i < 4; ++i) bias[nt][i] = blo(b1[f0 + (nt < 2 ? 8 * g + 4 * nt + i : 32 + 4 * g + i)]);
    __builtin_amdgcn_s_sleep(PRE3);
    const unsigned hb = WS_HT + dt * 262144 + wv * 65536, vo = g * 1024 + c * 16;
    u4 B[4][4];
#pragma unroll 1
    for (unsigned s = 0; s < SPIN_CAP; ++s) {
      FENCE();
      const unsigned can = __builtin_amdgcn_raw_buffer_load_b32(rws, lane * 1024 + 1020, hb, SC1);
#pragma unroll
      for (int kk = 0; kk < 4; ++kk)
#pragma unroll
        for (int mt = 0; mt < 4; ++mt) { B[kk][mt] = ldx(rws, vo + mt * 256, hb + kk * 4096); __builtin_amdgcn_sched_barrier(0); }
      if (!__builtin_amdgcn_ballot_w64(can == SENT)) break;
      for (; s < SPIN_CAP; ++s) { FENCE(); if (!__builtin_amdgcn_ballot_w64(__builtin_amdgcn_raw_buffer_load_b32(rws, lane * 1024 + 1020, hb, SC1) == SENT)) break; }
    }
    STAMP(7);
    f4 acc[3][4];
#pragma unroll
    for (int nt = 0; nt < 3; ++nt)
#pragma unroll
      for (int mt = 0; mt < 4; ++mt) acc[nt][mt] = Z;
    for (int n = 0; n < 4; ++n) {
      if (n < 3) wait1(L, 4, 4 * n + 8);
      const unsigned slot = a1off + (n < 2 ? (4 * n + 4) * 3072 : 0);
#pragma unroll
      for (int kk = 0; kk < 4; ++kk) {
        if (__builtin_expect(bad4(B[kk]), 0)) reload4(B[kk], rws, vo, hb + (4 * n + kk) * 4096);
#pragma unroll
        for (int mt = 0; mt < 4; ++mt)
#pragma unroll
          for (int nt = 0; nt < 3; ++nt) acc[nt][mt] = mfma_b(WA[kk][nt], B[kk][mt], acc[nt][mt]);
        if (n < 3) {
#pragma unroll
          for (int mt = 0; mt < 4; ++mt) B[kk][mt] = ldx(rws, vo + mt * 256, hb + (4 * n + kk + 4) * 4096);
#pragma unroll
          for (int nt = 0; nt < 3; ++nt) {
            const unsigned off = slot + kk * 3072 + (nt < 2 ? 16 * (lane & 3) + 8 * nt : 64 + 8 * (lane & 3));
            WA[kk][nt] = cat(trrd(L + off), trrd(L + off + 1536));
          }
        }
        __builtin_amdgcn_sched_barrier(0);
      }
      if (n == 2) sig(L, wv, 9);
    }
    asm volatile("s_nop 7\n\ts_nop 7" ::: "memory");
    STAMP(8);
#pragma unroll
    for (int mt = 0; mt < 4; ++mt)
#pragma unroll
      for (int nt = 0; nt < 3; ++nt) *(LAS f4*)(L + L_W + wv * L_WREG + (mt * 3 + nt) * 1024 + lane * 16) = acc[nt][mt];
    sig(L, wv, 10); wait4(L, 10);
    float gv[3][4];
#pragma unroll
    for (int nt = 0; nt < 3; ++nt) {
      f4 u = {bias[nt][0], bias[nt][1], bias[nt][2], bias[nt][3]};
#pragma unroll
      for (int v = 0; v < 4; ++v) u += *(LAS f4*)(L + L_W + v * L_WREG + (wv * 3 + nt) * 1024 + lane * 16);
#pragma unroll
      for (int i = 0; i < 4; ++i) {
        const float t = u[i];
        const float z = t * fmaf(t * t, -0.10294324f, -2.3022082f);
        gv[nt][i] = t * __builtin_amdgcn_rcpf(1.f + __builtin_amdgcn_exp2f(z));
      }
    }
    sig(L, wv, 11);
    const u4 ga = {pkb(gv[0][0], gv[0][1]), pkb(gv[0][2], gv[0][3]), pkb(gv[1][0], gv[1][1]), pkb(gv[1][2], gv[1][3])};
    const u2 gb = {min(pkb(gv[2][0], gv[2][1]), SENT - 1), min(pkb(gv[2][2], gv[2][3]), SENT - 1)};
    const unsigned gbase = WS_G + (dt * 172 + (f0 >> 3)) * 1024 + wv * 256;
    stx(clampS(ga), rws, g * 1024 + c * 16, gbase);
    __builtin_amdgcn_raw_buffer_store_b64(gb, rws, (4 + (g >> 1)) * 1024 + c * 16 + (g & 1) * 8, gbase, SC1);
  } else {
    sig(L, wv, 11);
  }
  STAMP(9);
  {
    const int srow = st * 64 + wv * 16 + c;
    const float bias = blo(b2[srow]);
    u4 WB[4][4];
    const int rho = 4 * g + ((lane >> 2) & 3);
    const unsigned b2off = L_W + wv * L_WREG + rho * 128 + 8 * (lane & 3), sw = (rho >> 1) & 3;
    wait1(L, 4, 20);
#pragma unroll
    for (int kk = 0; kk < 4; ++kk)
#pragma unroll
      for (int nt = 0; nt < 4; ++nt) { const unsigned off = b2off + 12288 + kk * 4096 + ((nt ^ sw) * 32); WB[kk][nt] = cat(trrd(L + off), trrd(L + off + 2048)); }
    sig(L, wv, 12);
    __builtin_amdgcn_s_sleep(PRE4);
    const int nks = wv == 3 ? 10 : 11;
    const unsigned gb = WS_G + (dt * 172 + wv * 44) * 1024, vo = g * 1024 + c * 16;
    const unsigned cano = min(lane, 4 * nks - 1) * 1024 + 1020;
    u4 A[4][4];
#pragma unroll 1
    for (unsigned s = 0; s < SPIN_CAP; ++s) {
      FENCE();
      const unsigned can = __builtin_amdgcn_raw_buffer_load_b32(rws, cano, gb, SC1);
#pragma unroll
      for (int kk = 0; kk < 4; ++kk)
#pragma unroll
        for (int mt = 0; mt < 4; ++mt) { A[kk][mt] = ldx(rws, vo + mt * 256, gb + kk * 4096); __builtin_amdgcn_sched_barrier(0); }
      if (!__builtin_amdgcn_ballot_w64(can == SENT)) break;
      for (; s < SPIN_CAP; ++s) { FENCE(); if (!__builtin_amdgcn_ballot_w64(__builtin_amdgcn_raw_buffer_load_b32(rws, cano, gb, SC1) == SENT)) break; }
    }
    STAMP(10);
    f4 acc[4][4];
#pragma unroll
    for (int mt = 0; mt < 4; ++mt)
#pragma unroll
      for (int nt = 0; nt < 4; ++nt) acc[mt][nt] = Z;
    u4 hv[2];
    for (int n = 0; n < 3; ++n) {
      if (n < 2) wait1(L, 4, n ? 27 : 25);
      if (n == 2) {
#pragma unroll
        for (int u = 0; u < 2; ++u) hv[u] = ldx(rws, c * 1024 + g * 16 + u * 64, WS_H + (st * 64 + wv * 16) * 1024 + dt * 128);
      }
#pragma unroll
      for (int kk = 0; kk < 4; ++kk) {
        const int ks = 4 * n + kk;
        if (ks < nks) {
          if (__builtin_expect(bad4(A[kk]), 0)) reload4(A[kk], rws, vo, gb + ks * 4096);
#pragma unroll
          for (int mt = 0; mt < 4; ++mt)
#pragma unroll
            for (int nt = 0; nt < 4; ++nt) acc[mt][nt] = mfma_b(A[kk][mt], WB[kk][nt], acc[mt][nt]);
          if (ks + 4 < nks) {
            const int k4 = ks + 4, so = k4 < 6 ? 12288 + k4 * 4096 : (k4 < 9 ? (k4 - 6) * 4096 : 12288 + (k4 - 9) * 4096);
#pragma unroll
            for (int mt = 0; mt < 4; ++mt) A[kk][mt] = ldx(rws, vo + mt * 256, gb + k4 * 4096);
#pragma unroll
            for (int nt = 0; nt < 4; ++nt) { const unsigned off = b2off + so + ((nt ^ sw) * 32); WB[kk][nt] = cat(trrd(L + off), trrd(L + off + 2048)); }
          }
        }
        __builtin_amdgcn_sched_barrier(0);
      }
    }
    asm volatile("s_nop 7\n\ts_nop 7" ::: "memory");
    STAMP(11);
    FENCE();
#pragma unroll
    for (int nt = 0; nt < 4; ++nt)
#pragma unroll
      for (int mt = 0; mt < 4; ++mt) *(LAS f4*)(L + L_W + wv * L_WREG + (nt * 4 + mt) * 1024 + lane * 16) = acc[mt][nt];
    sig(L, wv, 13);
    for (unsigned s = 0; s < SPIN_CAP && __builtin_amdgcn_ballot_w64(mx(mx(0u, hv[0]), hv[1]) == SENT); ++s) {
      FENCE();
#pragma unroll
      for (int u = 0; u < 2; ++u) hv[u] = ldx(rws, c * 1024 + g * 16 + u * 64, WS_H + (st * 64 + wv * 16) * 1024 + dt * 128);
    }
    wait4(L, 13);
#pragma unroll
    for (int u = 0; u < 2; ++u) {
      u4 ov;
#pragma unroll
      for (int hh = 0; hh < 2; ++hh) {
        f4 o = {bias, bias, bias, bias};
#pragma unroll
        for (int v = 0; v < 4; ++v) o += *(LAS f4*)(L + L_W + v * L_WREG + (wv * 4 + 2 * u + hh) * 1024 + lane * 16);
        ov[2 * hh] = pkb(o[0] + blo(hv[u][2 * hh]), o[1] + bhi(hv[u][2 * hh]));
        ov[2 * hh + 1] = pkb(o[2] + blo(hv[u][2 * hh + 1]), o[3] + bhi(hv[u][2 * hh + 1]));
      }
      *(u4*)(out + (size_t)srow * 512 + dt * 64 + u * 32 + g * 8) = ov;
    }
  }
}

extern "C" void kernel_launch(void* const* d_in, const int* in_sizes, int n_in, void* d_out, int out_size, void* d_ws, size_t ws_size,
                              hipStream_t stream) {
  (void)hipFuncSetAttribute((const void*)fused_kernel, hipFuncAttributeMaxDynamicSharedMemorySize, L_BYTES);
  (void)hipMemsetAsync(d_ws, 0xFF, WS_END, stream);
  fused_kernel<<<256, 320, L_BYTES, stream>>>((const unsigned short*)d_in[0], (const unsigned short*)d_in[1], (const unsigned short*)d_in[2],
                                              (const unsigned short*)d_in[4], (const unsigned short*)d_in[5], (const unsigned short*)d_in[6],
                                              (const unsigned short*)d_in[7], (unsigned short*)d_out, (unsigned char*)d_ws);
}
```

```cpp
#define CODEPF 0
#include <hip/hip_runtime.h>
#include <hip/hip_bf16.h>

typedef _Float16 h8 __attribute__((ext_vector_type(8)));
typedef _Float16 h2 __attribute__((ext_vector_type(2)));
typedef __bf16 b8 __attribute__((ext_vector_type(8)));
typedef __bf16 b2 __attribute__((ext_vector_type(2)));
typedef float f2 __attribute__((ext_vector_type(2)));
typedef float f4 __attribute__((ext_vector_type(4)));
typedef short s4 __attribute__((ext_vector_type(4)));
typedef unsigned u2 __attribute__((ext_vector_type(2)));
typedef unsigned u4 __attribute__((ext_vector_type(4)));
#define LAS __attribute__((address_space(3)))
#define DEV __device__ __forceinline__
#define FENCE() asm volatile("" ::: "memory")
#define PIN(x) asm volatile("" : "+v"(x))
#define DRAIN() asm volatile("s_waitcnt vmcnt(0)" ::: "memory")

#define STAMPS 0
#define PRE1 2
#define PRE2 2
#define PRE3 2
#define PRE4 2
#define WINL 9
#define PREW1 1
#define PREW1W 6
#define PREW2 4
#define ABL_WBYTES (2048 * 257 * 2)
#define ABL_W1BYTES (2048 * 1376 * 2)

#define CODE_BYTES 20480
constexpr int SC1 = 16;
constexpr unsigned SENT = 0xFFFFFFFFu, OOB = 0x7FFF0000u;
constexpr unsigned SPIN_CAP = 1u << 13;
constexpr unsigned WS_X1 = 0, WS_X2 = 2u << 20, WS_HT = 4u << 20, WS_H = 6u << 20, WS_G = 8u << 20;
constexpr unsigned WS_FLAG = 10u << 20, WS_SEED = WS_FLAG + 65536, WS_END = WS_SEED + 4096;
constexpr unsigned WS_STAMP = 16u << 20;
constexpr unsigned L_W = 0, L_WREG = 36864, L_E = 4 * L_WREG, L_A = L_E + 8192, L_BYTES = L_E + 16384;
constexpr unsigned L_FLAG = L_E + 1024, F_PROG = 192, F_HELP = 196, L_MEAN = L_FLAG + 256, L_PART = L_FLAG + 512, L_SINK = L_FLAG + 768;

struct Tabs { unsigned fwd[5][8]; unsigned inv[5][16]; };
constexpr unsigned short cs8(int n, unsigned short one, unsigned short rt) {
  n &= 7; if (n == 2 || n == 6) return 0; const unsigned short m = (n & 1) ? rt : one; return (n >= 3 && n <= 5) ? (unsigned short)(m | 0x8000) : m;
}
constexpr unsigned short comb(int w, int eo, int r, unsigned short one, unsigned short rt) { return w == 0 ? cs8(4 * eo * r, one, rt) : cs8(r * w - 2 * eo, one, rt); }
constexpr Tabs make_tabs() {
  Tabs t = {};
  for (int i = 0; i < 4; ++i) {
    for (int h = 0; h < 2; ++h) for (int j = 0; j < 4; ++j) { const int o = 4 * h + i;
      t.fwd[i][4 * h + j] = comb(o >> 1, o & 1, 2 * j, 0x3800, 0x35A8) | ((unsigned)comb(o >> 1, o & 1, 2 * j + 1, 0x3800, 0x35A8) << 16); }
    for (int h = 0; h < 2; ++h) for (int j = 0; j < 8; ++j) { const int r = 4 * h + i, w = j >> 1, tt = j & 1;
      unsigned e = comb(w, 0, r, 0x3B00, 0x3AB5), o = comb(w, 1, r, 0x3B00, 0x3AB5);
      if (w == 0) { if (tt) e = 0; else o = 0; }
      t.inv[i][8 * h + j] = e | (o << 16); }
  }
  return t;
}
__device__ const Tabs __attribute__((aligned(16))) TABS = make_tabs();

DEV f4 mfma_h(u4 a, u4 b, f4 c) { return __builtin_amdgcn_mfma_f32_16x16x32_f16(__builtin_bit_cast(h8, a), __builtin_bit_cast(h8, b), c, 0, 0, 0); }
DEV f4 mfma_b(u4 a, u4 b, f4 c) { return __builtin_amdgcn_mfma_f32_16x16x32_bf16(__builtin_bit_cast(b8, a), __builtin_bit_cast(b8, b), c, 0, 0, 0); }
DEV unsigned pkh(float a, float b) { f2 v = {a, b}; return __builtin_bit_cast(unsigned, __builtin_convertvector(v, h2)); }
DEV unsigned pkb(float a, float b) { f2 v = {a, b}; return __builtin_bit_cast(unsigned, __builtin_convertvector(v, b2)); }
DEV u4 ziph(f4 a, f4 b) { u4 r = {pkh(a[0], b[0]), pkh(a[1], b[1]), pkh(a[2], b[2]), pkh(a[3], b[3])}; return r; }
DEV u4 zipb(f4 a, f4 b) { u4 r = {pkb(a[0], b[0]), pkb(a[1], b[1]), pkb(a[2], b[2]), pkb(a[3], b[3])}; return r; }
DEV u4 catb(f4 a, f4 b) { u4 r = {pkb(a[0], a[1]), pkb(a[2], a[3]), pkb(b[0], b[1]), pkb(b[2], b[3])}; return r; }
DEV float blo(unsigned u) { return __builtin_bit_cast(float, u << 16); }
DEV float bhi(unsigned u) { return __builtin_bit_cast(float, u & 0xFFFF0000u); }
DEV f4 cos4(f4 r) { f4 o = {__builtin_amdgcn_cosf(r[0]), __builtin_amdgcn_cosf(r[1]), __builtin_amdgcn_cosf(r[2]), __builtin_amdgcn_cosf(r[3])}; return o; }
DEV f4 sin4(f4 r) { f4 o = {__builtin_amdgcn_sinf(r[0]), __builtin_amdgcn_sinf(r[1]), __builtin_amdgcn_sinf(r[2]), __builtin_amdgcn_sinf(r[3])}; return o; }
template <class T> DEV LAS T* lp(unsigned off) { return (LAS T*)(size_t)off; }
DEV u2 trrd(unsigned off) { return __builtin_bit_cast(u2, __builtin_amdgcn_ds_read_tr16_b64_v4i16(lp<s4>(off))); }
DEV u4 cat(u2 a, u2 b) { u4 r = {a.x, a.y, b.x, b.y}; return r; }
DEV unsigned mx(unsigned m, u4 v) { return max(max(max(m, v.x), v.y), max(v.z, v.w)); }
DEV bool anysent(unsigned m) { return __builtin_amdgcn_ballot_w64(m == SENT) != 0; }
DEV u4 ldx(__amdgpu_buffer_rsrc_t r, unsigned v, unsigned s) { return __builtin_amdgcn_raw_buffer_load_b128(r, v, s, SC1); }
DEV unsigned ldw(__amdgpu_buffer_rsrc_t r, unsigned v, unsigned s) { return __builtin_amdgcn_raw_buffer_load_b32(r, v, s, SC1); }
DEV void stx(u4 d, __amdgpu_buffer_rsrc_t r, unsigned v, unsigned s) {
  __builtin_amdgcn_raw_buffer_store_b128(d, r, v, s, SC1);
  asm volatile("s_nop 1" ::"v"(d) : "memory");
}
DEV void reload2(u4 (&v)[2], __amdgpu_buffer_rsrc_t r, unsigned vo, unsigned so) {
#pragma unroll 1
  for (unsigned s = 0; s < SPIN_CAP; ++s) { FENCE(); v[0] = ldx(r, vo, so); v[1] = ldx(r, vo + 256, so); if (!anysent(mx(mx(0u, v[0]), v[1]))) break; }
}
template <int C> DEV float ror_add(float v) { return v + __builtin_bit_cast(float, __builtin_amdgcn_update_dpp(0, __builtin_bit_cast(int, v), C, 0xF, 0xF, true)); }
DEV float row16_sum(float t) { t = ror_add<0x128>(t); t = ror_add<0x124>(t); t = ror_add<0x122>(t); return ror_add<0x121>(t); }
DEV f2 swap32(float a, float b) { auto r = __builtin_amdgcn_permlane32_swap(__builtin_bit_cast(unsigned, a), __builtin_bit_cast(unsigned, b), false, false); const unsigned r0 = r[0], r1 = r[1]; f2 o = {__builtin_bit_cast(float, r0), __builtin_bit_cast(float, r1)}; return o; }
DEV f2 swap16(float a, float b) { auto r = __builtin_amdgcn_permlane16_swap(__builtin_bit_cast(unsigned, a), __builtin_bit_cast(unsigned, b), false, false); const unsigned r0 = r[0], r1 = r[1]; f2 o = {__builtin_bit_cast(float, r0), __builtin_bit_cast(float, r1)}; return o; }
DEV void cmul(f4& r, f4& i, f4 c, f4 s) { const f4 t = r * c - i * s; i = r * s + i * c; r = t; }
DEV void cmulc(f4& r, f4& i, f4 c, f4 s) { const f4 t = r * c + i * s; i = i * c - r * s; r = t; }

struct Sync { unsigned base, mine, one; };
DEV void sig(const Sync& y, int slot) { FENCE(); *lp<volatile unsigned char>(y.mine + 8 * slot) = (unsigned char)y.one; FENCE(); }
DEV unsigned long long flagrd(const Sync& y, int slot) { FENCE(); const unsigned long long f = *lp<volatile unsigned long long>(y.base + 8 * slot); FENCE(); return f; }
DEV bool flagok(unsigned long long f) { FENCE(); return __builtin_amdgcn_ballot_w64(f != 0x0101010101010101ull) == 0; }
DEV void waitall(const Sync& y, int slot) { while (!flagok(flagrd(y, slot))) {} }
#define WAIT_READ(slot, ...) for (;;) { const unsigned long long f_ = flagrd(Y, slot); __VA_ARGS__; if (flagok(f_)) break; }
DEV void setw(const Sync& y, unsigned off, unsigned e) { FENCE(); *lp<volatile unsigned>(y.base + off) = e; FENCE(); }
DEV void waitw(const Sync& y, unsigned off, unsigned e) {
  for (;;) { FENCE(); const unsigned f = *lp<volatile unsigned>(y.base + off); FENCE(); if (__builtin_amdgcn_ballot_w64(f < e) == 0) break; }
}
DEV void waitall_slow(const Sync& y, int slot) { while (!flagok(flagrd(y, slot))) __builtin_amdgcn_s_sleep(1); }
DEV void dma16(__amdgpu_buffer_rsrc_t r, unsigned dst, unsigned v, unsigned s) { __builtin_amdgcn_raw_ptr_buffer_load_lds(r, lp<void>(dst), 16, v, s, 0, 0); }
DEV void touch(__amdgpu_buffer_rsrc_t r, unsigned v, unsigned s) { __builtin_amdgcn_raw_ptr_buffer_load_lds(r, lp<void>(L_SINK), 4, v, s, 0, 0); }
#define STAMP(i)

__global__ void __launch_bounds__(640, 3)
fused_kernel(const unsigned short* __restrict__ x, const unsigned short* __restrict__ w_r, const unsigned short* __restrict__ w_i,
             const unsigned short* __restrict__ w1, const unsigned short* __restrict__ b1, const unsigned short* __restrict__ w2,
             const unsigned short* __restrict__ b2, unsigned short* __restrict__ out, unsigned char* ws) {
  unsigned long long pc0; asm volatile("s_getpc_b64 %0" : "=s"(pc0)); pc0 &= ~127ull;
  asm volatile("" ::"s"(x), "s"(w_r), "s"(w_i), "s"(w1), "s"(b1), "s"(w2), "s"(b2), "s"(out), "s"(ws));
  const int tid = threadIdx.x, lane = tid & 63, u = __builtin_amdgcn_readfirstlane(tid >> 6);
  const int g = lane >> 4, c = lane & 15, q = (lane >> 2) & 3, p4 = lane & 3;
  const int xcd = blockIdx.x & 7, i32 = blockIdx.x >> 3, b = xcd * 32 + i32;
  const int s0 = (b & 0xC0) | ((b & 3) << 4) | ((b >> 2) & 15);
  const int dt = 4 * (xcd & 1) + (i32 & 3), fq = xcd >> 1;
  const int ft = (fq ? 1 + 7 * fq : 0) + (i32 >> 2);
  const bool g1 = (i32 >> 2) < (fq ? 7 : 8);
  const int f0 = min(48 * ft, 1328);
  const int st = 8 * fq + (i32 >> 2);
  STAMP(0);
  const __amdgpu_buffer_rsrc_t rws = __builtin_amdgcn_make_buffer_rsrc((void*)ws, 0, WS_END, 0x00020000);
  const u4 S4 = {SENT, SENT, SENT, SENT};
  Sync Y = {L_FLAG, L_FLAG + u, 1u};
  PIN(Y.base); PIN(Y.mine); PIN(Y.one);

  if (u >= 8) {
    if (u == 9) {
      const unsigned seed = ldw(rws, 0, WS_SEED);
      __builtin_amdgcn_s_barrier();
#pragma unroll
      for (int i = 0; i < 16; ++i) __builtin_amdgcn_raw_buffer_store_b128(S4, rws, lane * 16 + (i & 3) * 1024, (i >> 3) * WS_X2 + b * 8192 + (i & 4) * 1024, SC1);
      asm volatile("s_nop 1" ::"v"(S4) : "memory");
      const u2 tag = {seed + 1u, (seed + 1u) * 0x9E3779B1u ^ 0x5BD1E995u};
      DRAIN();
      if (lane == 0) __builtin_amdgcn_raw_buffer_store_b64(tag, rws, 0, WS_FLAG + b * 128, SC1);
      setw(Y, F_HELP, 1);
      STAMP(1);
      DRAIN();
#pragma unroll 1
      for (unsigned s = 0; s < SPIN_CAP; ++s) {
        FENCE();
        unsigned bad = 0;
#pragma unroll
        for (int i = 0; i < 4; ++i) { const u2 f = __builtin_amdgcn_raw_buffer_load_b64(rws, lane * 128, WS_FLAG + i * 8192, SC1); bad |= (f.x ^ tag.x) | (f.y ^ tag.y); }
        if (!__builtin_amdgcn_ballot_w64(bad != 0)) break;
      }
      setw(Y, F_HELP, 2);
      STAMP(2);
      if (b == 0 && lane == 0) __builtin_amdgcn_raw_buffer_store_b32(tag.x, rws, 0, WS_SEED, SC1);
      return;
    }
    *lp<volatile unsigned>(L_FLAG + 4 * lane) = 0u;
    asm volatile("s_waitcnt lgkmcnt(0)" ::: "memory");
    __builtin_amdgcn_s_barrier();
    const __amdgpu_buffer_rsrc_t r1 = __builtin_amdgcn_make_buffer_rsrc((void*)w1, 0, ABL_W1BYTES, 0x00020000);
    const __amdgpu_buffer_rsrc_t r2 = __builtin_amdgcn_make_buffer_rsrc((void*)w2, 0, 2048 * 1376 * 2, 0x00020000);
    {
      const int cb0 = 96 * (fq ? 1 + 7 * fq : 0), cb1 = min(cb0 + (fq ? 672 : 768), 2752) - 4;
      const unsigned tv = ((lane >> 5) + 16 * (4 * i32 + ((lane >> 3) & 3))) * 2752 + min(cb0 + 128 * (lane & 7), cb1);
#pragma unroll
      for (int n = 0; n < 8; ++n) touch(r1, tv, 2 * n * 2752);
      asm volatile("s_waitcnt vmcnt(%0)" ::"i"(PREW1W) : "memory");
    }
    if (g1) {
      unsigned vo[3];
#pragma unroll
      for (int n = 0; n < 3; ++n) {
        const int pc = 64 * n + lane, rho = pc / 6, pcol = pc - 6 * rho;
        const int gg = (rho >> 2) & 3, r = 4 * (rho >> 4) + (rho & 3);
        vo[n] = (16 * gg + 256 * r) * 2752 + f0 * 2 + pcol * 16;
      }
#pragma unroll 1
      for (int ks = 0; ks < 16; ++ks) {
        if (ks == 12) waitall_slow(Y, 6);
        if (ks >= PREW2 && ks < PREW2 + 6) touch(r2, min(43 * i32 + 8 * (ks - PREW2) + (lane >> 3), 43 * i32 + 42) * 4096 + (lane & 7) * 128, fq * 1024);
        const int slot = ks < 12 ? ks : ks - 12;
#pragma unroll
        for (int v = 0; v < 4; ++v) {
#pragma unroll
          for (int n = 0; n < 3; ++n) {
            dma16(r1, L_W + v * L_WREG + slot * 3072 + n * 1024, vo[n], (64 * v + ks) * 2752);
            asm volatile("s_waitcnt vmcnt(%0)" ::"i"(WINL) : "memory");
          }
        }
        setw(Y, F_PROG, ks);
      }
    }
    else {
      __builtin_amdgcn_s_sleep(64);
#pragma unroll 1
      for (int n = 0; n < 6; ++n) touch(r2, min(43 * i32 + 8 * n + (lane >> 3), 43 * i32 + 42) * 4096 + (lane & 7) * 128, fq * 1024);
    }
    DRAIN();
    setw(Y, F_PROG, 16);
    STAMP(1);
    unsigned vo2[4];
#pragma unroll
    for (int n = 0; n < 4; ++n) {
      const int rho = 8 * n + (lane >> 3), pp = lane & 7;
      const int fr = 8 * ((rho >> 2) & 3) + 4 * (rho >> 4) + (rho & 3);
      vo2[n] = fr * 4096 + st * 128 + ((pp ^ (((rho >> 1) & 3) << 1)) * 16);
    }
    waitall_slow(Y, 9);
#pragma unroll 1
    for (int ks = 0; ks < 11; ++ks) {
      if (ks == 6) { setw(Y, F_PROG, 20); waitall_slow(Y, 11); }
      if (ks == 9) waitall_slow(Y, 12);
      const int so = ks < 6 ? 12288 + ks * 4096 : (ks < 9 ? (ks - 6) * 4096 : 12288 + (ks - 9) * 4096);
#pragma unroll
      for (int v = 0; v < 4; ++v) {
#pragma unroll
        for (int n = 0; n < 4; ++n) dma16(r2, L_W + v * L_WREG + so + n * 1024, vo2[n], min(11 * v + ks, 42) * (32 * 4096));
      }
      asm volatile("s_waitcnt vmcnt(32)" ::: "memory");
    }
    setw(Y, F_PROG, 25);
    DRAIN();
    setw(Y, F_PROG, 27);
    return;
  }

  const __amdgpu_buffer_rsrc_t rx = __builtin_amdgcn_make_buffer_rsrc((void*)x, 0, 2048 * 1024, 0x00020000);
  u4 xa = __builtin_amdgcn_raw_buffer_load_b128(rx, lane * 16, (s0 + 256 * u) * 1024, 0);
  FENCE();
  __builtin_amdgcn_s_barrier();
  FENCE();
  const unsigned* tabf = TABS.fwd[(c >> 2) == g ? (c & 3) : 4];
  const u4 AC0 = *(const u4*)tabf, AC1 = *(const u4*)(tabf + 4);
  const int pw = b >> 6, pt = (b >> 5) & 1, pp2 = pt ? (pw ? 8 - pw : 4) : pw, k2b = (b >> 1) & 15;
  const bool mir = k2b >= 8, sym = pw && !pt, spec = (b & 1) == 0 && (!sym || (k2b & 7) == 0);
  const bool emir = pw && (pt ? mir : !mir);
  u4 wraw; unsigned wex;
  {
    const __amdgpu_buffer_rsrc_t rw = __builtin_amdgcn_make_buffer_rsrc((void*)(u < 4 ? w_r : w_i), 0, ABL_WBYTES, 0x00020000);
    const int j = pp2 + 8 * (tid & 255), jm = (2048 - j) & 2047, kb = 8 * (b & 1) + 32 * k2b;
    wraw = __builtin_amdgcn_raw_buffer_load_b128(rw, (mir ? jm : j) * 514, (mir ? 505 - kb : kb) * 2, 0);
    const int ke = pw == 0 ? 16 * (k2b ? k2b : 16) : (pt ? (mir ? 496 - kb : kb + 16) : kb);
    wex = __builtin_amdgcn_raw_buffer_load_b16(rw, (emir ? jm : j) * 514, spec ? ke * 2 : OOB, 0);
  }
  __builtin_amdgcn_raw_buffer_store_b128(S4, rws, tid * 16, WS_HT + b * 8192, SC1);
  __builtin_amdgcn_raw_buffer_store_b128(S4, rws, tid * 16, b < 172 ? WS_G + b * 8192 : OOB, SC1);
  stx(S4, rws, tid * 16, WS_H + b * 8192);
  const int w = u >> 1, t = u & 1, pu = t ? (w ? 8 - w : 4) : w;
  const float tp = (float)(s0 * pu) * (1.f / 2048.f);
  const f4 kf = {(float)(4 * g), (float)(4 * g + 1), (float)(4 * g + 2), (float)(4 * g + 3)};
  const f4 nf = kf * (float)c;
  const f4 c16 = cos4(nf * (1.f / 16.f)), s16 = sin4(nf * (1.f / 16.f));
  const f4 c32 = cos4(nf * (1.f / 32.f)), s32 = sin4(nf * (1.f / 32.f));
  u4 FR = ziph(0.25f * c16, 0.25f * s16), FI = ziph(-0.25f * s16, 0.25f * c16);
  const float A1 = (w == 0 && t) ? 0.f : 1.f, A2 = w ? (t ? -1.f : 1.f) : 0.f, A3 = 1.f - A1, A4 = -A2;
  const f4 Z = {0.f, 0.f, 0.f, 0.f};

  {
    const f4 alt = {1.f, -1.f, 1.f, -1.f};
    const f4 cc = 0.125f * ((w && t && c == 0) ? alt : c32), ns = 0.125f * ((w == 0 && c == 0) ? alt : -s32);
    u4 Mr0 = ziph(A1 * cc, A2 * ns + A3 * cc), Mi0 = ziph(A1 * ns, A4 * cc + A3 * ns);
    const unsigned sg = (c & 1) ? 0x80008000u : 0u;
    u4 Mr1 = Mr0 ^ sg, Mi1 = Mi0 ^ sg;
    const f4 n16 = c ? nf : 16.f * kf;
    f4 ang = (t ? n16 : nf) * (1.f / 512.f) + tp;
    if (w == 0 && c == 0) ang = Z;
    f4 tc = cos4(ang), ts = sin4(ang);
    u4 GR = {}, GI = {};
    if (w == 0) {
      const f4 ps = n16 * (1.f / 32.f) + tp, gc = 0.25f * cos4(ps), gs = -0.25f * sin4(ps);
      GR = (c & 1) ? ziph(Z, gc) : ziph(gc, Z); GI = (c & 1) ? ziph(Z, gs) : ziph(gs, Z);
    }
    unsigned aXW = L_A + u * 1024 + ((16 * lane) ^ ((u & 3) << 6));
    unsigned aXR = L_A + q * 1024 + ((128 * u + 32 * g + 8 * p4) ^ (q << 6));
    unsigned aEW = L_A + (c >> 3) * 1024 + 128 * u + (c & 7) * 16 + 4 * g;
    unsigned aEW2 = aEW ^ 128;
    unsigned aER = L_A + (2 * w + (c >> 3)) * 1024 + ((128 * g) ^ ((w & 1) << 7)) + (c & 7) * 16;
    unsigned aST = (c * 4 + g) * 16;
    PIN(Mr0); PIN(Mi0); PIN(Mr1); PIN(Mi1); PIN(tc); PIN(ts); PIN(GR); PIN(GI); PIN(FR); PIN(FI);
    PIN(aXW); PIN(aXR); PIN(aEW); PIN(aEW2); PIN(aER); PIN(aST);
    STAMP(13);
    PIN(xa);
    const f4 va = {blo(xa[0]), bhi(xa[0]), blo(xa[1]), bhi(xa[1])}, vb = {blo(xa[2]), bhi(xa[2]), blo(xa[3]), bhi(xa[3])};
    const f4 s4v = va + vb, q4v = va * va + vb * vb;
    const f2 sq = swap32((s4v[0] + s4v[2]) + (s4v[1] + s4v[3]), (q4v[0] + q4v[2]) + (q4v[1] + q4v[3]));
    float tt = sq.x + sq.y;
    const f2 t16 = swap16(tt, tt);
    tt = row16_sum(t16.x + t16.y);
    const f2 tot = swap32(tt, tt);
    const float mean = tot.x * (1.f / 512.f);
    const float rs = __builtin_amdgcn_rsqf(fmaxf(fmaf(-mean, mean, tot.y * (1.f / 512.f)), 0.f) + 1e-5f), mr = -mean * rs;
    const f4 na = va * rs + mr, nb = vb * rs + mr;
    const u4 xn = {pkh(na[0], na[1]), pkh(na[2], na[3]), pkh(nb[0], nb[1]), pkh(nb[2], nb[3])};
    *lp<u4>(aXW) = xn;
    sig(Y, 1);
    u4 bf;
    WAIT_READ(1, bf = cat(trrd(aXR), trrd(aXR + 4096)); PIN(bf));
    const f4 d0 = mfma_h(AC0, bf, Z), d1 = mfma_h(AC1, bf, Z);
    *lp<unsigned>(aEW) = pkh(d0[0], d0[1]);
    *lp<unsigned>(aEW2 + 2048) = pkh(d0[2], d0[3]);
    *lp<unsigned>(aEW + 4096) = pkh(d1[0], d1[1]);
    *lp<unsigned>(aEW2 + 6144) = pkh(d1[2], d1[3]);
    sig(Y, 2);
    const unsigned hl = *lp<volatile unsigned>(Y.base + F_HELP);
    u4 e0, e1;
    WAIT_READ(2, e0 = *lp<u4>(aER); e1 = *lp<u4>(aER + 512); PIN(e0); PIN(e1));
    f4 zr = mfma_h(e1, Mr1, mfma_h(e0, Mr0, Z)), zi = mfma_h(e1, Mi1, mfma_h(e0, Mi0, Z));
    cmulc(zr, zi, tc, ts);
    const u4 zp = ziph(zr, zi);
    const f4 gr = mfma_h(zp, GR, Z), gi = mfma_h(zp, GI, Z);
    f4 yr = mfma_h(zp, FR, Z), yi = mfma_h(zp, FI, Z);
    const bool sel = w == 0 && g == 0;
    yr[0] = sel ? gr[0] : yr[0]; yi[0] = sel ? gi[0] : yi[0];
    u4 ov = ziph(yr, yi);
    PIN(ov);
    if (__builtin_expect(__builtin_amdgcn_readfirstlane(hl) == 0, 0)) waitw(Y, F_HELP, 1);
    stx(ov, rws, aST, WS_X1 + b * 8192 + u * 1024);
    sig(Y, 3);
    *lp<float>(L_MEAN + 4 * u) = mean;
  }
  STAMP(2);
  u4 IR = zipb(c16, -s16), II = zipb(s16, c16);
  const int dn = 32 * (g >> 1) + 8 * (c >> 2) + 4 * (g & 1) + (c & 3);
  unsigned short xr[8];
#pragma unroll
  for (int r = 0; r < 8; ++r) xr[r] = __builtin_amdgcn_raw_buffer_load_b16(rx, dn * 2, (s0 + 256 * r) * 1024 + 128 * u, 0);
  {
    f4 tc = cos4(nf * (1.f / 256.f)), ts = sin4(nf * (1.f / 256.f));
    f4 wre, wim;
    if (spec) {
      if (u >= 4 && emir != mir) wex ^= 0x8000u;
      if (sym) wex = pkb(0.5f * (blo(wex) + (mir ? bhi(wraw[3]) : blo(wraw[0]))), 0.f);
      if (mir) wraw[3] = (wraw[3] & 0xFFFFu) | (wex << 16); else wraw[0] = (wraw[0] & 0xFFFF0000u) | wex;
    }
    waitall(Y, 3);
    *lp<u4>(L_A + tid * 16) = wraw;
    sig(Y, 15); waitall(Y, 15);
    {
      const unsigned pr = L_A + c * 16 + g * 1024 + 2 * (mir ? 7 - u : u);
#pragma unroll
      for (int i = 0; i < 4; ++i) { wre[i] = blo(*lp<unsigned short>(pr + i * 256)); wim[i] = blo(*lp<unsigned short>(pr + i * 256 + 4096)); }
      if (mir) wim = -wim;
    }
    sig(Y, 16);
    unsigned aLD = (tid >> 1) * 8192 + (tid & 1) * 16;
    unsigned aPW = L_A + (tid & 1) * 4096 + (((tid >> 1) * 4) ^ ((tid & 1) << 6));
    unsigned aPR = L_A + u * 1024 + ((16 * lane) ^ ((u >> 2) << 6));
    unsigned aST = tid * 16;
    PIN(tc); PIN(ts); PIN(wre); PIN(wim); PIN(IR); PIN(II); PIN(aLD); PIN(aPW); PIN(aPR); PIN(aST);
    DRAIN();
    waitall(Y, 16);
    waitw(Y, F_HELP, 2);
    __builtin_amdgcn_s_sleep(PRE1);
    STAMP(14);
    u4 v;
#pragma unroll 1
    for (unsigned s = 0; s < SPIN_CAP; ++s) { FENCE(); v = ldx(rws, aLD, WS_X1 + b * 32); if (!anysent(mx(0u, v))) break; }
    STAMP(3);
#pragma unroll
    for (int i = 0; i < 4; ++i) *lp<unsigned>(aPW + i * 1024) = v[i];
    sig(Y, 4);
    u4 a;
    WAIT_READ(4, a = *lp<u4>(aPR); PIN(a));
    f4 r = mfma_h(a, FR, Z), im = mfma_h(a, FI, Z);
    cmulc(r, im, tc, ts);
    u4 z = ziph(r, im);
    r = mfma_h(FR, z, Z); im = mfma_h(FI, z, Z);
    cmul(r, im, wre, wim);
    z = zipb(r, im);
    r = mfma_b(z, IR, Z); im = mfma_b(z, II, Z);
    cmul(r, im, tc, ts);
    z = zipb(r, im);
    r = mfma_b(IR, z, Z); im = mfma_b(II, z, Z);
    *lp<u4>(aPR) = zipb(r, im);
    sig(Y, 5);
    u4 ov;
    WAIT_READ(5, ov[0] = *lp<unsigned>(aPW); ov[1] = *lp<unsigned>(aPW + 1024); ov[2] = *lp<unsigned>(aPW + 2048); ov[3] = *lp<unsigned>(aPW + 3072); PIN(ov));
    stx(ov, rws, aST, WS_X2 + b * 8192);
  }
  STAMP(4);
  u4 WA[4][3];
  const unsigned a1off = L_W + w * L_WREG + (4 * g + q) * 96;
  if (g1) {
    waitw(Y, F_PROG, 4);
#pragma unroll
    for (int kk = 0; kk < 4; ++kk)
#pragma unroll
      for (int nt = 0; nt < 3; ++nt) {
        const unsigned off = a1off + kk * 3072 + (nt < 2 ? 16 * p4 + 8 * nt : 64 + 8 * p4);
        WA[kk][nt] = cat(trrd(off), trrd(off + 1536));
      }
  }
  sig(Y, 6);
  {
    const unsigned* tabi = TABS.inv[(c >> 2) == g ? (c & 3) : 4];
    u4 AI00 = *(const u4*)tabi, AI01 = *(const u4*)(tabi + 4), AI10 = *(const u4*)(tabi + 8), AI11 = *(const u4*)(tabi + 12);
    const f4 m0 = *lp<f4>(L_MEAN), m1 = *lp<f4>(L_MEAN + 16);
    f4 xc0 = {blo(xr[0]), blo(xr[1]), blo(xr[2]), blo(xr[3])}, xc1 = {blo(xr[4]), blo(xr[5]), blo(xr[6]), blo(xr[7])};
    xc0 -= m0; xc1 -= m1;
    f4 n16 = nf; if (g == 0) n16[0] = 16.f * (float)c;
    f4 ang = (t ? n16 : nf) * (1.f / 512.f) + tp;
    if (w == 0 && g == 0) ang[0] = 0.f;
    f4 tc = cos4(ang), ts = sin4(ang);
    u4 GR = {}, GI = {};
    if (w == 0) {
      const f4 ps = n16 * (1.f / 32.f) + tp; f4 gc = cos4(ps), gs = -sin4(ps);
      if (g == 0) { gc[0] *= 0.5f; gs[0] *= 0.5f; }
      const u4 gg = zipb(gc, gs);
      GR[0] = gg[0]; GR[2] = gg[2]; GI[1] = gg[1]; GI[3] = gg[3];
    }
    f4 cc = c32, ns = -s32;
    { const float alt = (c & 1) ? -1.f : 1.f; if (g == 0) { if (w && t) cc[0] = alt; if (w == 0) ns[0] = alt; } }
    u4 Me0 = zipb(A1 * cc, A1 * ns), Mo0 = zipb(A2 * ns + A3 * cc, A4 * cc + A3 * ns);
    const u4 SG = {0u, 0x80008000u, 0u, 0x80008000u};
    u4 Me1 = Me0 ^ SG, Mo1 = Mo0 ^ SG;
    unsigned aLD = (8 * g + (c >> 3)) * 8192 + (c & 7) * 4;
    const int re_ = 2 * u, ro_ = u ? 2 * u + 1 : 2;
    const unsigned wo = 32 * c + 8 * (g ^ (c >> 2));
    unsigned aEWe = L_E + re_ * 1024 + (wo ^ ((re_ & 3) << 6)), aEWo = L_E + ro_ * 1024 + (wo ^ ((ro_ & 3) << 6));
    const unsigned ro = 128 * u + 64 * (g >> 1) + 32 * (p4 >> 1) + 8 * ((2 * (p4 & 1) + (g & 1)) ^ (u & 3));
    unsigned aER = L_E + q * 1024 + (ro ^ (q << 6)), aER0 = q == 1 ? aER + 1024 : aER;
    unsigned aPW = L_PART + u * 32 + (lane >> 3) * 4, aPR = L_PART + lane * 4, aST = lane * 16;
    PIN(AI00); PIN(AI01); PIN(AI10); PIN(AI11); PIN(xc0); PIN(xc1); PIN(tc); PIN(ts); PIN(GR); PIN(GI); PIN(Me0); PIN(Mo0); PIN(Me1); PIN(Mo1);
    PIN(aLD); PIN(aEWe); PIN(aEWo); PIN(aER); PIN(aER0); PIN(aPW); PIN(aPR); PIN(aST);
    DRAIN();
    waitall(Y, 6);
    __builtin_amdgcn_s_sleep(PRE2);
    STAMP(15);
    u4 z;
#pragma unroll 1
    for (unsigned s = 0; s < SPIN_CAP; ++s) {
      FENCE();
#pragma unroll
      for (int i = 0; i < 4; ++i) z[i] = ldw(rws, aLD, WS_X2 + b * 32 + (u * 32 + 2 * i) * 8192);
      if (!anysent(mx(0u, z))) break;
    }
    STAMP(5);
    const f4 gr = mfma_b(z, GR, Z), gi = mfma_b(z, GI, Z);
    f4 r = mfma_b(z, IR, Z), im = mfma_b(z, II, Z);
    const bool sel = w == 0 && g == 0;
    r[0] = sel ? gr[0] : r[0]; im[0] = sel ? gi[0] : im[0];
    cmul(r, im, tc, ts);
    const u4 U = zipb(r, im);
    const f4 e0 = mfma_b(U, Me0, Z), o0 = mfma_b(U, Mo0, Z), e1 = mfma_b(U, Me1, Z), o1 = mfma_b(U, Mo1, Z);
    const u4 pe = catb(e0, e1), po = catb(o0, o1);
    *lp<u2>(aEWe) = pe.xy; *lp<u2>(aEWo) = po.xy;
    *lp<u2>(aEWe + 512) = pe.zw; *lp<u2>(aEWo + 512) = po.zw;
    sig(Y, 7);
    u4 k0, k1;
    WAIT_READ(7, k0 = cat(trrd(aER0), trrd(aER + 4096)); k1 = cat(trrd(aER + 8192), trrd(aER + 12288)); PIN(k0); PIN(k1));
    const f4 h0 = mfma_b(AI01, k1, mfma_b(AI00, k0, xc0)), h1 = mfma_b(AI11, k1, mfma_b(AI10, k0, xc1));
    const f4 q0 = h0 * h0, q1 = h1 * h1;
    const f2 a0 = swap32(q0[0], q1[0]), a1 = swap32(q0[1], q1[1]), a2 = swap32(q0[2], q1[2]), a3 = swap32(q0[3], q1[3]);
    const f2 b0 = swap16(a0.x + a0.y, a2.x + a2.y), b1 = swap16(a1.x + a1.y, a3.x + a3.y);
    float pr;
    asm volatile("s_nop 1\n\tv_add_f32_dpp %0, %1, %1 row_ror:8 row_mask:0xf bank_mask:0x3\n\tv_add_f32_dpp %0, %2, %2 row_ror:8 row_mask:0xf bank_mask:0xc\n\t"
                 "s_nop 1\n\tv_add_f32_dpp %0, %0, %0 row_half_mirror row_mask:0xf bank_mask:0xf\n\t"
                 "s_nop 1\n\tv_add_f32_dpp %0, %0, %0 quad_perm:[2,3,0,1] row_mask:0xf bank_mask:0xf\n\t"
                 "s_nop 1\n\tv_add_f32_dpp %0, %0, %0 quad_perm:[1,0,3,2] row_mask:0xf bank_mask:0xf"
                 : "=&v"(pr) : "v"(b0.x + b0.y), "v"(b1.x + b1.y));
    *lp<float>(aPW) = pr;
    sig(Y, 8);
    float ps;
    WAIT_READ(8, ps = *lp<float>(aPR); PIN(ps));
    ps = ror_add<0x128>(ps);
    const f2 p16 = swap16(ps, ps); ps = p16.x + p16.y;
    const f2 p32 = swap32(ps, ps); ps = p32.x + p32.y;
    const int rsv = __builtin_bit_cast(int, __builtin_amdgcn_rsqf(fmaf(ps, (1.f + 1e-5f) / 512.f, 1e-10f)));
    f4 r0, r1;
#pragma unroll
    for (int i = 0; i < 4; ++i) { r0[i] = __builtin_bit_cast(float, __builtin_amdgcn_readlane(rsv, i)); r1[i] = __builtin_bit_cast(float, __builtin_amdgcn_readlane(rsv, 4 + i)); }
    stx(catb(h0 * r0, h1 * r1), rws, aST, WS_HT + u * 262144 + b * 1024);
    STAMP(6);
    FENCE();
    *lp<u4>(L_A + u * 1024 + dn * 16) = catb(h0 + m0, h1 + m1);
    FENCE();
    const unsigned ph = L_A + u * 1024 + (16 * g + 8 * (p4 >> 1) + q) * 16 + 8 * (p4 & 1);
    stx(cat(trrd(ph), trrd(ph + 64)), rws, (c & 7) * 262144 + (2 * g + (c >> 3)) * 16, WS_H + s0 * 1024 + u * 128);
  }
  const unsigned vo = g * 1024 + c * 16 + t * 512;
  if (g1) {
    u4 braw;
    if (u < 4) braw = *(const u4*)(b1 + f0 + 8 * g); else braw.xy = *(const u2*)(b1 + f0 + 32 + 4 * g);
    DRAIN();
    __builtin_amdgcn_s_sleep(PRE3);
    const unsigned hb = WS_HT + dt * 262144 + w * 65536, cano = lane * 1024 + t * 512 + 508;
    u4 B[4][2];
#pragma unroll 1
    for (unsigned s = 0; s < SPIN_CAP; ++s) {
      FENCE();
      const unsigned can = ldw(rws, cano, hb);
#pragma unroll
      for (int kk = 0; kk < 4; ++kk)
#pragma unroll
        for (int mt = 0; mt < 2; ++mt) { B[kk][mt] = ldx(rws, vo + mt * 256, hb + kk * 4096); __builtin_amdgcn_sched_barrier(0); }
      if (!anysent(can)) break;
#pragma unroll 1
      for (; s < SPIN_CAP; ++s) { FENCE(); if (!anysent(ldw(rws, cano, hb))) break; }
    }
    STAMP(7);
    f4 acc[3][2];
#pragma unroll
    for (int nt = 0; nt < 3; ++nt) { acc[nt][0] = Z; acc[nt][1] = Z; }
#pragma unroll
    for (int n = 0; n < 4; ++n) {
      if (n < 3) waitw(Y, F_PROG, 4 * n + 8);
      const unsigned slot = a1off + (n < 2 ? (4 * n + 4) * 3072 : 0);
#pragma unroll
      for (int kk = 0; kk < 4; ++kk) {
        if (__builtin_expect(anysent(mx(mx(0u, B[kk][0]), B[kk][1])), 0)) reload2(B[kk], rws, vo, hb + (4 * n + kk) * 4096);
#pragma unroll
        for (int mt = 0; mt < 2; ++mt)
#pragma unroll
          for (int nt = 0; nt < 3; ++nt) acc[nt][mt] = mfma_b(WA[kk][nt], B[kk][mt], acc[nt][mt]);
        if (n < 3) {
#pragma unroll
          for (int mt = 0; mt < 2; ++mt) B[kk][mt] = ldx(rws, vo + mt * 256, hb + (4 * n + kk + 4) * 4096);
#pragma unroll
          for (int nt = 0; nt < 3; ++nt) {
            const unsigned off = slot + kk * 3072 + (nt < 2 ? 16 * p4 + 8 * nt : 64 + 8 * p4);
            WA[kk][nt] = cat(trrd(off), trrd(off + 1536));
          }
        }
        if (n == 3 && kk == 2) waitall(Y, 9);
        __builtin_amdgcn_sched_barrier(0);
      }
      if (n == 2) sig(Y, 9);
    }
    STAMP(8);
#pragma unroll
    for (int mt = 0; mt < 2; ++mt)
#pragma unroll
      for (int nt = 0; nt < 3; ++nt) *lp<f4>(L_W + w * L_WREG + ((2 * t + mt) * 3 + nt) * 1024 + lane * 16) = acc[nt][mt];
    sig(Y, 10);
    const unsigned gbase = WS_G + (dt * 172 + (f0 >> 3)) * 1024 + (u & 3) * 256;
    const unsigned pr = L_W + ((u & 3) * 3 + (u >> 2) * 2) * 1024 + lane * 16;
    auto gelu = [](f4 v) {
      const f4 z = v * (v * v * -0.10294324f - 2.3022082f);
      f4 e = {__builtin_amdgcn_exp2f(z[0]), __builtin_amdgcn_exp2f(z[1]), __builtin_amdgcn_exp2f(z[2]), __builtin_amdgcn_exp2f(z[3])};
      e += 1.f;
      const f4 r = {__builtin_amdgcn_rcpf(e[0]), __builtin_amdgcn_rcpf(e[1]), __builtin_amdgcn_rcpf(e[2]), __builtin_amdgcn_rcpf(e[3])};
      return v * r;
    };
    const f4 bA = {blo(braw[0]), bhi(braw[0]), blo(braw[1]), bhi(braw[1])};
    if (u < 4) {
      const f4 bB = {blo(braw[2]), bhi(braw[2]), blo(braw[3]), bhi(braw[3])};
      f4 pa[4], pb[4];
      waitall(Y, 10);
#pragma unroll
      for (int v = 0; v < 4; ++v) { pa[v] = *lp<f4>(pr + v * L_WREG); pb[v] = *lp<f4>(pr + v * L_WREG + 1024); }
      sig(Y, 11);
      stx(catb(gelu(((pa[0] + bA) + pa[1]) + (pa[2] + pa[3])), gelu(((pb[0] + bB) + pb[1]) + (pb[2] + pb[3]))), rws, g * 1024 + c * 16, gbase);
    } else {
      f4 pa[4];
      waitall(Y, 10);
#pragma unroll
      for (int v = 0; v < 4; ++v) pa[v] = *lp<f4>(pr + v * L_WREG);
      sig(Y, 11);
      const f4 gv = gelu(((pa[0] + bA) + pa[1]) + (pa[2] + pa[3]));
      const u2 gb = {pkb(gv[0], gv[1]), pkb(gv[2], gv[3])};
      __builtin_amdgcn_raw_buffer_store_b64(gb, rws, (4 + (g >> 1)) * 1024 + c * 16 + (g & 1) * 8, gbase, SC1);
    }
  } else {
    sig(Y, 9); sig(Y, 11);
    __builtin_amdgcn_s_sleep(100);
  }
  STAMP(9);
  {
    const int srow = st * 64 + w * 16 + c;
    const float bias = blo(b2[srow]);
    const unsigned hvo = c * 1024 + g * 16, hso = WS_H + (st * 64 + w * 16) * 1024 + dt * 128 + t * 64;
    u4 hv = ldx(rws, hvo, hso);
    u4 WB[2][4];
    const int rho = 4 * g + q;
    const unsigned b2off = L_W + w * L_WREG + rho * 128 + 8 * p4, sw = ((rho >> 1) & 3) ^ w;
    waitw(Y, F_PROG, 20);
#pragma unroll
    for (int kk = 0; kk < 2; ++kk)
#pragma unroll
      for (int nt = 0; nt < 4; ++nt) { const unsigned off = b2off + 12288 + kk * 4096 + ((nt ^ sw) * 32); WB[kk][nt] = cat(trrd(off), trrd(off + 2048)); }
    sig(Y, 12);
    DRAIN();
#pragma unroll 1
    for (unsigned s = 0; s < SPIN_CAP && anysent(mx(0u, hv)); ++s) { FENCE(); hv = ldx(rws, hvo, hso); }
    f4 ha = {blo(hv[0]) + bias, bhi(hv[0]) + bias, blo(hv[1]) + bias, bhi(hv[1]) + bias}, hb4 = {blo(hv[2]) + bias, bhi(hv[2]) + bias, blo(hv[3]) + bias, bhi(hv[3]) + bias};
    PIN(ha); PIN(hb4);
    __builtin_amdgcn_s_sleep(PRE4);
    const unsigned gb = WS_G + (dt * 172 + w * 44) * 1024, glast = gb + (w == 3 ? 9 : 10) * 4096;
    const unsigned cano = min(lane, w == 3 ? 39 : 43) * 1024 + t * 512 + 508;
    u4 A[4][2];
#pragma unroll 1
    for (unsigned s = 0; s < SPIN_CAP; ++s) {
      FENCE();
      const unsigned can = ldw(rws, cano, gb);
#pragma unroll
      for (int kk = 0; kk < 4; ++kk)
#pragma unroll
        for (int mt = 0; mt < 2; ++mt) { A[kk][mt] = ldx(rws, vo + mt * 256, gb + kk * 4096); __builtin_amdgcn_sched_barrier(0); }
      if (!anysent(can)) break;
#pragma unroll 1
      for (; s < SPIN_CAP; ++s) { FENCE(); if (!anysent(ldw(rws, cano, gb))) break; }
    }
    STAMP(10);
    f4 acc[2][4];
#pragma unroll
    for (int nt = 0; nt < 4; ++nt) { acc[0][nt] = Z; acc[1][nt] = Z; }
#pragma unroll
    for (int ks = 0; ks < 11; ++ks) {
      const int kk = ks & 3;
      if (ks == 2) waitw(Y, F_PROG, 25);
      if (ks == 7) waitw(Y, F_PROG, 27);
      if (ks == 10) waitall(Y, 13);
      if (__builtin_expect(anysent(mx(mx(0u, A[kk][0]), A[kk][1])), 0)) reload2(A[kk], rws, vo, ks == 10 ? glast : gb + ks * 4096);
      if (ks == 10) { const unsigned km = w == 3 ? 0u : ~0u; A[kk][0] &= km; A[kk][1] &= km; }
#pragma unroll
      for (int mt = 0; mt < 2; ++mt)
#pragma unroll
        for (int nt = 0; nt < 4; ++nt) acc[mt][nt] = mfma_b(A[kk][mt], WB[ks & 1][nt], acc[mt][nt]);
      if (ks + 4 < 11) {
#pragma unroll
        for (int mt = 0; mt < 2; ++mt) A[kk][mt] = ldx(rws, vo + mt * 256, ks + 4 == 10 ? glast : gb + (ks + 4) * 4096);
      }
      if (ks + 2 < 11) {
        const int k4 = ks + 2, so = k4 < 6 ? 12288 + k4 * 4096 : (k4 < 9 ? (k4 - 6) * 4096 : 12288 + (k4 - 9) * 4096);
#pragma unroll
        for (int nt = 0; nt < 4; ++nt) { const unsigned off = b2off + so + ((nt ^ sw) * 32); WB[ks & 1][nt] = cat(trrd(off), trrd(off + 2048)); }
      }
      if (ks == 8) sig(Y, 13);
      __builtin_amdgcn_sched_barrier(0);
    }
    STAMP(11);
#pragma unroll
    for (int nt = 1; nt < 4; ++nt)
#pragma unroll
      for (int mt = 0; mt < 2; ++mt) *lp<f4>(L_W + w * L_WREG + (nt * 4 + 2 * t + mt) * 1024 + lane * 16) = acc[mt][nt];
    sig(Y, 14);
    ha += acc[0][0]; hb4 += acc[1][0];
    f4 pa[4], pb[4];
    waitall(Y, 14);
#pragma unroll
    for (int v = 1; v < 4; ++v) { const unsigned pr = L_W + (w ^ v) * L_WREG + (v * 4 + 2 * t) * 1024 + lane * 16; pa[v] = *lp<f4>(pr); pb[v] = *lp<f4>(pr + 1024); }
    *(u4*)(out + (size_t)srow * 512 + dt * 64 + t * 32 + g * 8) = catb((ha + pa[1]) + (pa[2] + pa[3]), (hb4 + pb[1]) + (pb[2] + pb[3]));
  }
}

extern "C" void kernel_launch(void* const* d_in, const int* in_sizes, int n_in, void* d_out, int out_size, void* d_ws, size_t ws_size,
                              hipStream_t stream) {
  (void)hipFuncSetAttribute((const void*)fused_kernel, hipFuncAttributeMaxDynamicSharedMemorySize, L_BYTES);
  fused_kernel<<<256, 640, L_BYTES, stream>>>((const unsigned short*)d_in[0], (const unsigned short*)d_in[1], (const unsigned short*)d_in[2],
                                              (const unsigned short*)d_in[4], (const unsigned short*)d_in[5], (const unsigned short*)d_in[6],
                                              (const unsigned short*)d_in[7], (unsigned short*)d_out, (unsigned char*)d_ws);
}
```

```cpp
#define TSTAMP 2
#include <hip/hip_runtime.h>
#include <hip/hip_bf16.h>

typedef _Float16 h8 __attribute__((ext_vector_type(8)));
typedef _Float16 h2 __attribute__((ext_vector_type(2)));
typedef __bf16 b8 __attribute__((ext_vector_type(8)));
typedef __bf16 b2 __attribute__((ext_vector_type(2)));
typedef float f2 __attribute__((ext_vector_type(2)));
typedef float f4 __attribute__((ext_vector_type(4)));
typedef short s4 __attribute__((ext_vector_type(4)));
typedef unsigned u2 __attribute__((ext_vector_type(2)));
typedef unsigned u4 __attribute__((ext_vector_type(4)));
#define LAS __attribute__((address_space(3)))
#define DEV __device__ __forceinline__
#define FENCE() asm volatile("" ::: "memory")
#define PIN(x) asm volatile("" : "+v"(x))
#define DRAIN() asm volatile("s_waitcnt vmcnt(0)" ::: "memory")

#define STAMPS 0
#define PRE1 2
#define PRE2 2
#define PRE3 2
#define PRE4 2
#define WINL 9
#define PREW1 1
#define PREW1W 6
#define PREW2 4
#define ABL_WBYTES (2048 * 257 * 2)
#define ABL_W1BYTES (2048 * 1376 * 2)
#define TBLK -1
#define CODEPF 1

#define CODE_BYTES 20480
constexpr int SC1 = 16;
constexpr unsigned SENT = 0xFFFFFFFFu, OOB = 0x7FFF0000u;
constexpr unsigned SPIN_CAP = 1u << 13;
constexpr unsigned WS_X1 = 0, WS_X2 = 2u << 20, WS_HT = 4u << 20, WS_H = 6u << 20, WS_G = 8u << 20;
constexpr unsigned WS_FLAG = 10u << 20, WS_SEED = WS_FLAG + 65536, WS_END = WS_SEED + 4096;
constexpr unsigned WS_STAMP = 16u << 20;
constexpr unsigned L_W = 0, L_WREG = 36864, L_E = 4 * L_WREG, L_A = L_E + 8192, L_BYTES = L_E + 16384;
constexpr unsigned L_FLAG = L_E + 1024, F_PROG = 192, F_HELP = 196, L_MEAN = L_FLAG + 256, L_PART = L_FLAG + 512, L_SINK = L_FLAG + 768;

struct Tabs { unsigned fwd[5][8]; unsigned inv[5][16]; };
constexpr unsigned short cs8(int n, unsigned short one, unsigned short rt) {
  n &= 7; if (n == 2 || n == 6) return 0; const unsigned short m = (n & 1) ? rt : one; return (n >= 3 && n <= 5) ? (unsigned short)(m | 0x8000) : m;
}
constexpr unsigned short comb(int w, int eo, int r, unsigned short one, unsigned short rt) { return w == 0 ? cs8(4 * eo * r, one, rt) : cs8(r * w - 2 * eo, one, rt); }
constexpr Tabs make_tabs() {
  Tabs t = {};
  for (int i = 0; i < 4; ++i) {
    for (int h = 0; h < 2; ++h) for (int j = 0; j < 4; ++j) { const int o = 4 * h + i;
      t.fwd[i][4 * h + j] = comb(o >> 1, o & 1, 2 * j, 0x3800, 0x35A8) | ((unsigned)comb(o >> 1, o & 1, 2 * j + 1, 0x3800, 0x35A8) << 16); }
    for (int h = 0; h < 2; ++h) for (int j = 0; j < 8; ++j) { const int r = 4 * h + i, w = j >> 1, tt = j & 1;
      unsigned e = comb(w, 0, r, 0x3B00, 0x3AB5), o = comb(w, 1, r, 0x3B00, 0x3AB5);
      if (w == 0) { if (tt) e = 0; else o = 0; }
      t.inv[i][8 * h + j] = e | (o << 16); }
  }
  return t;
}
__device__ const Tabs __attribute__((aligned(16))) TABS = make_tabs();

DEV f4 mfma_h(u4 a, u4 b, f4 c) { return __builtin_amdgcn_mfma_f32_16x16x32_f16(__builtin_bit_cast(h8, a), __builtin_bit_cast(h8, b), c, 0, 0, 0); }
DEV f4 mfma_b(u4 a, u4 b, f4 c) { return __builtin_amdgcn_mfma_f32_16x16x32_bf16(__builtin_bit_cast(b8, a), __builtin_bit_cast(b8, b), c, 0, 0, 0); }
DEV unsigned pkh(float a, float b) { f2 v = {a, b}; return __builtin_bit_cast(unsigned, __builtin_convertvector(v, h2)); }
DEV unsigned pkb(float a, float b) { f2 v = {a, b}; return __builtin_bit_cast(unsigned, __builtin_convertvector(v, b2)); }
DEV u4 ziph(f4 a, f4 b) { u4 r = {pkh(a[0], b[0]), pkh(a[1], b[1]), pkh(a[2], b[2]), pkh(a[3], b[3])}; return r; }
DEV u4 zipb(f4 a, f4 b) { u4 r = {pkb(a[0], b[0]), pkb(a[1], b[1]), pkb(a[2], b[2]), pkb(a[3], b[3])}; return r; }
DEV u4 catb(f4 a, f4 b) { u4 r = {pkb(a[0], a[1]), pkb(a[2], a[3]), pkb(b[0], b[1]), pkb(b[2], b[3])}; return r; }
DEV float blo(unsigned u) { return __builtin_bit_cast(float, u << 16); }
DEV float bhi(unsigned u) { return __builtin_bit_cast(float, u & 0xFFFF0000u); }
DEV f4 cos4(f4 r) { f4 o = {__builtin_amdgcn_cosf(r[0]), __builtin_amdgcn_cosf(r[1]), __builtin_amdgcn_cosf(r[2]), __builtin_amdgcn_cosf(r[3])}; return o; }
DEV f4 sin4(f4 r) { f4 o = {__builtin_amdgcn_sinf(r[0]), __builtin_amdgcn_sinf(r[1]), __builtin_amdgcn_sinf(r[2]), __builtin_amdgcn_sinf(r[3])}; return o; }
template <class T> DEV LAS T* lp(unsigned off) { return (LAS T*)(size_t)off; }
DEV u2 trrd(unsigned off) { return __builtin_bit_cast(u2, __builtin_amdgcn_ds_read_tr16_b64_v4i16(lp<s4>(off))); }
DEV u4 cat(u2 a, u2 b) { u4 r = {a.x, a.y, b.x, b.y}; return r; }
DEV unsigned mx(unsigned m, u4 v) { return max(max(max(m, v.x), v.y), max(v.z, v.w)); }
DEV bool anysent(unsigned m) { return __builtin_amdgcn_ballot_w64(m == SENT) != 0; }
DEV u4 ldx(__amdgpu_buffer_rsrc_t r, unsigned v, unsigned s) { return __builtin_amdgcn_raw_buffer_load_b128(r, v, s, SC1); }
DEV unsigned ldw(__amdgpu_buffer_rsrc_t r, unsigned v, unsigned s) { return __builtin_amdgcn_raw_buffer_load_b32(r, v, s, SC1); }
DEV void stx(u4 d, __amdgpu_buffer_rsrc_t r, unsigned v, unsigned s) {
  __builtin_amdgcn_raw_buffer_store_b128(d, r, v, s, SC1);
  asm volatile("s_nop 1" ::"v"(d) : "memory");
}
DEV void reload2(u4 (&v)[2], __amdgpu_buffer_rsrc_t r, unsigned vo, unsigned so) {
#pragma unroll 1
  for (unsigned s = 0; s < SPIN_CAP; ++s) { FENCE(); v[0] = ldx(r, vo, so); v[1] = ldx(r, vo + 256, so); if (!anysent(mx(mx(0u, v[0]), v[1]))) break; }
}
template <int C> DEV float ror_add(float v) { return v + __builtin_bit_cast(float, __builtin_amdgcn_update_dpp(0, __builtin_bit_cast(int, v), C, 0xF, 0xF, true)); }
DEV float row16_sum(float t) { t = ror_add<0x128>(t); t = ror_add<0x124>(t); t = ror_add<0x122>(t); return ror_add<0x121>(t); }
DEV f2 swap32(float a, float b) { auto r = __builtin_amdgcn_permlane32_swap(__builtin_bit_cast(unsigned, a), __builtin_bit_cast(unsigned, b), false, false); const unsigned r0 = r[0], r1 = r[1]; f2 o = {__builtin_bit_cast(float, r0), __builtin_bit_cast(float, r1)}; return o; }
DEV f2 swap16(float a, float b) { auto r = __builtin_amdgcn_permlane16_swap(__builtin_bit_cast(unsigned, a), __builtin_bit_cast(unsigned, b), false, false); const unsigned r0 = r[0], r1 = r[1]; f2 o = {__builtin_bit_cast(float, r0), __builtin_bit_cast(float, r1)}; return o; }
DEV void cmul(f4& r, f4& i, f4 c, f4 s) { const f4 t = r * c - i * s; i = r * s + i * c; r = t; }
DEV void cmulc(f4& r, f4& i, f4 c, f4 s) { const f4 t = r * c + i * s; i = i * c - r * s; r = t; }

struct Sync { unsigned base, mine, one; };
DEV void sig(const Sync& y, int slot) { FENCE(); *lp<volatile unsigned char>(y.mine + 8 * slot) = (unsigned char)y.one; FENCE(); }
DEV unsigned long long flagrd(const Sync& y, int slot) { FENCE(); const unsigned long long f = *lp<volatile unsigned long long>(y.base + 8 * slot); FENCE(); return f; }
DEV bool flagok(unsigned long long f) { FENCE(); return __builtin_amdgcn_ballot_w64(f != 0x0101010101010101ull) == 0; }
DEV void waitall(const Sync& y, int slot) { while (!flagok(flagrd(y, slot))) {} }
#define WAIT_READ(slot, ...) for (;;) { const unsigned long long f_ = flagrd(Y, slot); __VA_ARGS__; if (flagok(f_)) break; }
DEV void setw(const Sync& y, unsigned off, unsigned e) { FENCE(); *lp<volatile unsigned>(y.base + off) = e; FENCE(); }
DEV void waitw(const Sync& y, unsigned off, unsigned e) {
  for (;;) { FENCE(); const unsigned f = *lp<volatile unsigned>(y.base + off); FENCE(); if (__builtin_amdgcn_ballot_w64(f < e) == 0) break; }
}
DEV void waitall_slow(const Sync& y, int slot) { while (!flagok(flagrd(y, slot))) __builtin_amdgcn_s_sleep(1); }
DEV void dma16(__amdgpu_buffer_rsrc_t r, unsigned dst, unsigned v, unsigned s) { __builtin_amdgcn_raw_ptr_buffer_load_lds(r, lp<void>(dst), 16, v, s, 0, 0); }
DEV void touch(__amdgpu_buffer_rsrc_t r, unsigned v, unsigned s) { __builtin_amdgcn_raw_ptr_buffer_load_lds(r, lp<void>(L_SINK), 4, v, s, 0, 0); }
DEV unsigned long long rtc() { unsigned long long t_; asm volatile("s_memrealtime %0\n\ts_waitcnt lgkmcnt(0)" : "=s"(t_) :: "memory"); return t_; }
#define STAMP(i) do { if (i == 0) ts0 = rtc(); if (i == TSTAMP) ts1 = rtc(); } while (0)

__global__ void __launch_bounds__(640, 3)
fused_kernel(const unsigned short* __restrict__ x, const unsigned short* __restrict__ w_r, const unsigned short* __restrict__ w_i,
             const unsigned short* __restrict__ w1, const unsigned short* __restrict__ b1, const unsigned short* __restrict__ w2,
             const unsigned short* __restrict__ b2, unsigned short* __restrict__ out, unsigned char* ws) {
  unsigned long long pc0; asm volatile("s_getpc_b64 %0" : "=s"(pc0)); pc0 &= ~127ull;
  asm volatile("" ::"s"(x), "s"(w_r), "s"(w_i), "s"(w1), "s"(b1), "s"(w2), "s"(b2), "s"(out), "s"(ws));
  const int tid = threadIdx.x, lane = tid & 63, u = __builtin_amdgcn_readfirstlane(tid >> 6);
  const int g = lane >> 4, c = lane & 15, q = (lane >> 2) & 3, p4 = lane & 3;
  const int xcd = blockIdx.x & 7, i32 = blockIdx.x >> 3, b = xcd * 32 + i32;
  const int s0 = (b & 0xC0) | ((b & 3) << 4) | ((b >> 2) & 15);
  const int dt = 4 * (xcd & 1) + (i32 & 3), fq = xcd >> 1;
  const int ft = (fq ? 1 + 7 * fq : 0) + (i32 >> 2);
  const bool g1 = (i32 >> 2) < (fq ? 7 : 8);
  const int f0 = min(48 * ft, 1328);
  const int st = 8 * fq + (i32 >> 2);
  unsigned long long ts0 = 0, ts1 = 0;
  STAMP(0);
  const __amdgpu_buffer_rsrc_t rws = __builtin_amdgcn_make_buffer_rsrc((void*)ws, 0, WS_END, 0x00020000);
  const u4 S4 = {SENT, SENT, SENT, SENT};
  Sync Y = {L_FLAG, L_FLAG + u, 1u};
  PIN(Y.base); PIN(Y.mine); PIN(Y.one);

  if (u >= 8) {
    if (u == 9) {
      const unsigned seed = ldw(rws, 0, WS_SEED);
      __builtin_amdgcn_s_barrier();
#pragma unroll
      for (int i = 0; i < 16; ++i) __builtin_amdgcn_raw_buffer_store_b128(S4, rws, lane * 16 + (i & 3) * 1024, (i >> 3) * WS_X2 + b * 8192 + (i & 4) * 1024, SC1);
      asm volatile("s_nop 1" ::"v"(S4) : "memory");
      const u2 tag = {seed + 1u, (seed + 1u) * 0x9E3779B1u ^ 0x5BD1E995u};
      DRAIN();
      if (lane == 0) __builtin_amdgcn_raw_buffer_store_b64(tag, rws, 0, WS_FLAG + b * 128, SC1);
      setw(Y, F_HELP, 1);
      STAMP(1);
      DRAIN();
#pragma unroll 1
      for (unsigned s = 0; s < SPIN_CAP; ++s) {
        FENCE();
        unsigned bad = 0;
#pragma unroll
        for (int i = 0; i < 4; ++i) { const u2 f = __builtin_amdgcn_raw_buffer_load_b64(rws, lane * 128, WS_FLAG + i * 8192, SC1); bad |= (f.x ^ tag.x) | (f.y ^ tag.y); }
        if (!__builtin_amdgcn_ballot_w64(bad != 0)) break;
      }
      setw(Y, F_HELP, 2);
      STAMP(2);
      if (b == 0 && lane == 0) __builtin_amdgcn_raw_buffer_store_b32(tag.x, rws, 0, WS_SEED, SC1);
      return;
    }
    __builtin_amdgcn_global_load_lds((const __attribute__((address_space(1))) void*)(pc0 + min(((i32 & 3) * 64 + lane) * 128, CODE_BYTES - 128)), lp<void>(L_SINK), 4, 0, 0);
    *lp<volatile unsigned>(L_FLAG + 4 * lane) = 0u;
    asm volatile("s_waitcnt lgkmcnt(0)" ::: "memory");
    __builtin_amdgcn_s_barrier();
    const __amdgpu_buffer_rsrc_t r1 = __builtin_amdgcn_make_buffer_rsrc((void*)w1, 0, ABL_W1BYTES, 0x00020000);
    const __amdgpu_buffer_rsrc_t r2 = __builtin_amdgcn_make_buffer_rsrc((void*)w2, 0, 2048 * 1376 * 2, 0x00020000);
    {
      const int cb0 = 96 * (fq ? 1 + 7 * fq : 0), cb1 = min(cb0 + (fq ? 672 : 768), 2752) - 4;
      const unsigned tv = ((lane >> 5) + 16 * (4 * i32 + ((lane >> 3) & 3))) * 2752 + min(cb0 + 128 * (lane & 7), cb1);
#pragma unroll
      for (int n = 0; n < 8; ++n) touch(r1, tv, 2 * n * 2752);
      asm volatile("s_waitcnt vmcnt(%0)" ::"i"(PREW1W) : "memory");
    }
    if (g1) {
      unsigned vo[3];
#pragma unroll
      for (int n = 0; n < 3; ++n) {
        const int pc = 64 * n + lane, rho = pc / 6, pcol = pc - 6 * rho;
        const int gg = (rho >> 2) & 3, r = 4 * (rho >> 4) + (rho & 3);
        vo[n] = (16 * gg + 256 * r) * 2752 + f0 * 2 + pcol * 16;
      }
#pragma unroll 1
      for (int ks = 0; ks < 16; ++ks) {
        if (ks == 12) waitall_slow(Y, 6);
        if (ks >= PREW2 && ks < PREW2 + 6) touch(r2, min(43 * i32 + 8 * (ks - PREW2) + (lane >> 3), 43 * i32 + 42) * 4096 + (lane & 7) * 128, fq * 1024);
        const int slot = ks < 12 ? ks : ks - 12;
#pragma unroll
        for (int v = 0; v < 4; ++v) {
#pragma unroll
          for (int n = 0; n < 3; ++n) {
            dma16(r1, L_W + v * L_WREG + slot * 3072 + n * 1024, vo[n], (64 * v + ks) * 2752);
            asm volatile("s_waitcnt vmcnt(%0)" ::"i"(WINL) : "memory");
          }
        }
        setw(Y, F_PROG, ks);
      }
    }
    else {
      __builtin_amdgcn_s_sleep(64);
#pragma unroll 1
      for (int n = 0; n < 6; ++n) touch(r2, min(43 * i32 + 8 * n + (lane >> 3), 43 * i32 + 42) * 4096 + (lane & 7) * 128, fq * 1024);
    }
    DRAIN();
    setw(Y, F_PROG, 16);
    STAMP(1);
    unsigned vo2[4];
#pragma unroll
    for (int n = 0; n < 4; ++n) {
      const int rho = 8 * n + (lane >> 3), pp = lane & 7;
      const int fr = 8 * ((rho >> 2) & 3) + 4 * (rho >> 4) + (rho & 3);
      vo2[n] = fr * 4096 + st * 128 + ((pp ^ (((rho >> 1) & 3) << 1)) * 16);
    }
    waitall_slow(Y, 9);
#pragma unroll 1
    for (int ks = 0; ks < 11; ++ks) {
      if (ks == 6) { setw(Y, F_PROG, 20); waitall_slow(Y, 11); }
      if (ks == 9) waitall_slow(Y, 12);
      const int so = ks < 6 ? 12288 + ks * 4096 : (ks < 9 ? (ks - 6) * 4096 : 12288 + (ks - 9) * 4096);
#pragma unroll
      for (int v = 0; v < 4; ++v) {
#pragma unroll
        for (int n = 0; n < 4; ++n) dma16(r2, L_W + v * L_WREG + so + n * 1024, vo2[n], min(11 * v + ks, 42) * (32 * 4096));
      }
      asm volatile("s_waitcnt vmcnt(32)" ::: "memory");
    }
    setw(Y, F_PROG, 25);
    DRAIN();
    setw(Y, F_PROG, 27);
    return;
  }

  const __amdgpu_buffer_rsrc_t rx = __builtin_amdgcn_make_buffer_rsrc((void*)x, 0, 2048 * 1024, 0x00020000);
  u4 xa = __builtin_amdgcn_raw_buffer_load_b128(rx, lane * 16, (s0 + 256 * u) * 1024, 0);
  FENCE();
  __builtin_amdgcn_s_barrier();
  FENCE();
  const unsigned* tabf = TABS.fwd[(c >> 2) == g ? (c & 3) : 4];
  const u4 AC0 = *(const u4*)tabf, AC1 = *(const u4*)(tabf + 4);
  const int pw = b >> 6, pt = (b >> 5) & 1, pp2 = pt ? (pw ? 8 - pw : 4) : pw, k2b = (b >> 1) & 15;
  const bool mir = k2b >= 8, sym = pw && !pt, spec = (b & 1) == 0 && (!sym || (k2b & 7) == 0);
  const bool emir = pw && (pt ? mir : !mir);
  u4 wraw; unsigned wex;
  {
    const __amdgpu_buffer_rsrc_t rw = __builtin_amdgcn_make_buffer_rsrc((void*)(u < 4 ? w_r : w_i), 0, ABL_WBYTES, 0x00020000);
    const int j = pp2 + 8 * (tid & 255), jm = (2048 - j) & 2047, kb = 8 * (b & 1) + 32 * k2b;
    wraw = __builtin_amdgcn_raw_buffer_load_b128(rw, (mir ? jm : j) * 514, (mir ? 505 - kb : kb) * 2, 0);
    const int ke = pw == 0 ? 16 * (k2b ? k2b : 16) : (pt ? (mir ? 496 - kb : kb + 16) : kb);
    wex = __builtin_amdgcn_raw_buffer_load_b16(rw, (emir ? jm : j) * 514, spec ? ke * 2 : OOB, 0);
  }
  __builtin_amdgcn_raw_buffer_store_b128(S4, rws, tid * 16, WS_HT + b * 8192, SC1);
  __builtin_amdgcn_raw_buffer_store_b128(S4, rws, tid * 16, b < 172 ? WS_G + b * 8192 : OOB, SC1);
  stx(S4, rws, tid * 16, WS_H + b * 8192);
  const int w = u >> 1, t = u & 1, pu = t ? (w ? 8 - w : 4) : w;
  const float tp = (float)(s0 * pu) * (1.f / 2048.f);
  const f4 kf = {(float)(4 * g), (float)(4 * g + 1), (float)(4 * g + 2), (float)(4 * g + 3)};
  const f4 nf = kf * (float)c;
  const f4 c16 = cos4(nf * (1.f / 16.f)), s16 = sin4(nf * (1.f / 16.f));
  const f4 c32 = cos4(nf * (1.f / 32.f)), s32 = sin4(nf * (1.f / 32.f));
  u4 FR = ziph(0.25f * c16, 0.25f * s16), FI = ziph(-0.25f * s16, 0.25f * c16);
  const float A1 = (w == 0 && t) ? 0.f : 1.f, A2 = w ? (t ? -1.f : 1.f) : 0.f, A3 = 1.f - A1, A4 = -A2;
  const f4 Z = {0.f, 0.f, 0.f, 0.f};

  {
    const f4 alt = {1.f, -1.f, 1.f, -1.f};
    const f4 cc = 0.125f * ((w && t && c == 0) ? alt : c32), ns = 0.125f * ((w == 0 && c == 0) ? alt : -s32);
    u4 Mr0 = ziph(A1 * cc, A2 * ns + A3 * cc), Mi0 = ziph(A1 * ns, A4 * cc + A3 * ns);
    const unsigned sg = (c & 1) ? 0x80008000u : 0u;
    u4 Mr1 = Mr0 ^ sg, Mi1 = Mi0 ^ sg;
    const f4 n16 = c ? nf : 16.f * kf;
    f4 ang = (t ? n16 : nf) * (1.f / 512.f) + tp;
    if (w == 0 && c == 0) ang = Z;
    f4 tc = cos4(ang), ts = sin4(ang);
    u4 GR = {}, GI = {};
    if (w == 0) {
      const f4 ps = n16 * (1.f / 32.f) + tp, gc = 0.25f * cos4(ps), gs = -0.25f * sin4(ps);
      GR = (c & 1) ? ziph(Z, gc) : ziph(gc, Z); GI = (c & 1) ? ziph(Z, gs) : ziph(gs, Z);
    }
    unsigned aXW = L_A + u * 1024 + ((16 * lane) ^ ((u & 3) << 6));
    unsigned aXR = L_A + q * 1024 + ((128 * u + 32 * g + 8 * p4) ^ (q << 6));
    unsigned aEW = L_A + (c >> 3) * 1024 + 128 * u + (c & 7) * 16 + 4 * g;
    unsigned aEW2 = aEW ^ 128;
    unsigned aER = L_A + (2 * w + (c >> 3)) * 1024 + ((128 * g) ^ ((w & 1) << 7)) + (c & 7) * 16;
    unsigned aST = (c * 4 + g) * 16;
    PIN(Mr0); PIN(Mi0); PIN(Mr1); PIN(Mi1); PIN(tc); PIN(ts); PIN(GR); PIN(GI); PIN(FR); PIN(FI);
    PIN(aXW); PIN(aXR); PIN(aEW); PIN(aEW2); PIN(aER); PIN(aST);
    STAMP(13);
    PIN(xa);
    const f4 va = {blo(xa[0]), bhi(xa[0]), blo(xa[1]), bhi(xa[1])}, vb = {blo(xa[2]), bhi(xa[2]), blo(xa[3]), bhi(xa[3])};
    { f4 tmp = va; PIN(tmp); } STAMP(1);
    const f4 s4v = va + vb, q4v = va * va + vb * vb;
    const f2 sq = swap32((s4v[0] + s4v[2]) + (s4v[1] + s4v[3]), (q4v[0] + q4v[2]) + (q4v[1] + q4v[3]));
    float tt = sq.x + sq.y;
    const f2 t16 = swap16(tt, tt);
    tt = row16_sum(t16.x + t16.y);
    const f2 tot = swap32(tt, tt);
    const float mean = tot.x * (1.f / 512.f);
    const float rs = __builtin_amdgcn_rsqf(fmaxf(fmaf(-mean, mean, tot.y * (1.f / 512.f)), 0.f) + 1e-5f), mr = -mean * rs;
    const f4 na = va * rs + mr, nb = vb * rs + mr;
    const u4 xn = {pkh(na[0], na[1]), pkh(na[2], na[3]), pkh(nb[0], nb[1]), pkh(nb[2], nb[3])};
    *lp<u4>(aXW) = xn;
    sig(Y, 1);
    u4 bf;
    WAIT_READ(1, bf = cat(trrd(aXR), trrd(aXR + 4096)); PIN(bf));
    const f4 d0 = mfma_h(AC0, bf, Z), d1 = mfma_h(AC1, bf, Z);
    *lp<unsigned>(aEW) = pkh(d0[0], d0[1]);
    *lp<unsigned>(aEW2 + 2048) = pkh(d0[2], d0[3]);
    *lp<unsigned>(aEW + 4096) = pkh(d1[0], d1[1]);
    *lp<unsigned>(aEW2 + 6144) = pkh(d1[2], d1[3]);
    sig(Y, 2);
    const unsigned hl = *lp<volatile unsigned>(Y.base + F_HELP);
    u4 e0, e1;
    WAIT_READ(2, e0 = *lp<u4>(aER); e1 = *lp<u4>(aER + 512); PIN(e0); PIN(e1));
    f4 zr = mfma_h(e1, Mr1, mfma_h(e0, Mr0, Z)), zi = mfma_h(e1, Mi1, mfma_h(e0, Mi0, Z));
    cmulc(zr, zi, tc, ts);
    const u4 zp = ziph(zr, zi);
    const f4 gr = mfma_h(zp, GR, Z), gi = mfma_h(zp, GI, Z);
    f4 yr = mfma_h(zp, FR, Z), yi = mfma_h(zp, FI, Z);
    const bool sel = w == 0 && g == 0;
    yr[0] = sel ? gr[0] : yr[0]; yi[0] = sel ? gi[0] : yi[0];
    u4 ov = ziph(yr, yi);
    PIN(ov);
    if (__builtin_expect(__builtin_amdgcn_readfirstlane(hl) == 0, 0)) waitw(Y, F_HELP, 1);
    stx(ov, rws, aST, WS_X1 + b * 8192 + u * 1024);
    sig(Y, 3);
    *lp<float>(L_MEAN + 4 * u) = mean;
  }
  STAMP(2);
  u4 IR = zipb(c16, -s16), II = zipb(s16, c16);
  const int dn = 32 * (g >> 1) + 8 * (c >> 2) + 4 * (g & 1) + (c & 3);
  unsigned short xr[8];
#pragma unroll
  for (int r = 0; r < 8; ++r) xr[r] = __builtin_amdgcn_raw_buffer_load_b16(rx, dn * 2, (s0 + 256 * r) * 1024 + 128 * u, 0);
  {
    f4 tc = cos4(nf * (1.f / 256.f)), ts = sin4(nf * (1.f / 256.f));
    f4 wre, wim;
    if (spec) {
      if (u >= 4 && emir != mir) wex ^= 0x8000u;
      if (sym) wex = pkb(0.5f * (blo(wex) + (mir ? bhi(wraw[3]) : blo(wraw[0]))), 0.f);
      if (mir) wraw[3] = (wraw[3] & 0xFFFFu) | (wex << 16); else wraw[0] = (wraw[0] & 0xFFFF0000u) | wex;
    }
    waitall(Y, 3);
    *lp<u4>(L_A + tid * 16) = wraw;
    sig(Y, 15); waitall(Y, 15);
    {
      const unsigned pr = L_A + c * 16 + g * 1024 + 2 * (mir ? 7 - u : u);
#pragma unroll
      for (int i = 0; i < 4; ++i) { wre[i] = blo(*lp<unsigned short>(pr + i * 256)); wim[i] = blo(*lp<unsigned short>(pr + i * 256 + 4096)); }
      if (mir) wim = -wim;
    }
    sig(Y, 16);
    unsigned aLD = (tid >> 1) * 8192 + (tid & 1) * 16;
    unsigned aPW = L_A + (tid & 1) * 4096 + (((tid >> 1) * 4) ^ ((tid & 1) << 6));
    unsigned aPR = L_A + u * 1024 + ((16 * lane) ^ ((u >> 2) << 6));
    unsigned aST = tid * 16;
    PIN(tc); PIN(ts); PIN(wre); PIN(wim); PIN(IR); PIN(II); PIN(aLD); PIN(aPW); PIN(aPR); PIN(aST);
    DRAIN();
    waitall(Y, 16);
    waitw(Y, F_HELP, 2);
    __builtin_amdgcn_s_sleep(PRE1);
    STAMP(14);
    u4 v;
#pragma unroll 1
    for (unsigned s = 0; s < SPIN_CAP; ++s) { FENCE(); v = ldx(rws, aLD, WS_X1 + b * 32); if (!anysent(mx(0u, v))) break; }
    STAMP(3);
#pragma unroll
    for (int i = 0; i < 4; ++i) *lp<unsigned>(aPW + i * 1024) = v[i];
    sig(Y, 4);
    u4 a;
    WAIT_READ(4, a = *lp<u4>(aPR); PIN(a));
    f4 r = mfma_h(a, FR, Z), im = mfma_h(a, FI, Z);
    cmulc(r, im, tc, ts);
    u4 z = ziph(r, im);
    r = mfma_h(FR, z, Z); im = mfma_h(FI, z, Z);
    cmul(r, im, wre, wim);
    z = zipb(r, im);
    r = mfma_b(z, IR, Z); im = mfma_b(z, II, Z);
    cmul(r, im, tc, ts);
    z = zipb(r, im);
    r = mfma_b(IR, z, Z); im = mfma_b(II, z, Z);
    *lp<u4>(aPR) = zipb(r, im);
    sig(Y, 5);
    u4 ov;
    WAIT_READ(5, ov[0] = *lp<unsigned>(aPW); ov[1] = *lp<unsigned>(aPW + 1024); ov[2] = *lp<unsigned>(aPW + 2048); ov[3] = *lp<unsigned>(aPW + 3072); PIN(ov));
    stx(ov, rws, aST, WS_X2 + b * 8192);
  }
  STAMP(4);
  u4 WA[4][3];
  const unsigned a1off = L_W + w * L_WREG + (4 * g + q) * 96;
  if (g1) {
    waitw(Y, F_PROG, 4);
#pragma unroll
    for (int kk = 0; kk < 4; ++kk)
#pragma unroll
      for (int nt = 0; nt < 3; ++nt) {
        const unsigned off = a1off + kk * 3072 + (nt < 2 ? 16 * p4 + 8 * nt : 64 + 8 * p4);
        WA[kk][nt] = cat(trrd(off), trrd(off + 1536));
      }
  }
  sig(Y, 6);
  {
    const unsigned* tabi = TABS.inv[(c >> 2) == g ? (c & 3) : 4];
    u4 AI00 = *(const u4*)tabi, AI01 = *(const u4*)(tabi + 4), AI10 = *(const u4*)(tabi + 8), AI11 = *(const u4*)(tabi + 12);
    const f4 m0 = *lp<f4>(L_MEAN), m1 = *lp<f4>(L_MEAN + 16);
    f4 xc0 = {blo(xr[0]), blo(xr[1]), blo(xr[2]), blo(xr[3])}, xc1 = {blo(xr[4]), blo(xr[5]), blo(xr[6]), blo(xr[7])};
    xc0 -= m0; xc1 -= m1;
    f4 n16 = nf; if (g == 0) n16[0] = 16.f * (float)c;
    f4 ang = (t ? n16 : nf) * (1.f / 512.f) + tp;
    if (w == 0 && g == 0) ang[0] = 0.f;
    f4 tc = cos4(ang), ts = sin4(ang);
    u4 GR = {}, GI = {};
    if (w == 0) {
      const f4 ps = n16 * (1.f / 32.f) + tp; f4 gc = cos4(ps), gs = -sin4(ps);
      if (g == 0) { gc[0] *= 0.5f; gs[0] *= 0.5f; }
      const u4 gg = zipb(gc, gs);
      GR[0] = gg[0]; GR[2] = gg[2]; GI[1] = gg[1]; GI[3] = gg[3];
    }
    f4 cc = c32, ns = -s32;
    { const float alt = (c & 1) ? -1.f : 1.f; if (g == 0) { if (w && t) cc[0] = alt; if (w == 0) ns[0] = alt; } }
    u4 Me0 = zipb(A1 * cc, A1 * ns), Mo0 = zipb(A2 * ns + A3 * cc, A4 * cc + A3 * ns);
    const u4 SG = {0u, 0x80008000u, 0u, 0x80008000u};
    u4 Me1 = Me0 ^ SG, Mo1 = Mo0 ^ SG;
    unsigned aLD = (8 * g + (c >> 3)) * 8192 + (c & 7) * 4;
    const int re_ = 2 * u, ro_ = u ? 2 * u + 1 : 2;
    const unsigned wo = 32 * c + 8 * (g ^ (c >> 2));
    unsigned aEWe = L_E + re_ * 1024 + (wo ^ ((re_ & 3) << 6)), aEWo = L_E + ro_ * 1024 + (wo ^ ((ro_ & 3) << 6));
    const unsigned ro = 128 * u + 64 * (g >> 1) + 32 * (p4 >> 1) + 8 * ((2 * (p4 & 1) + (g & 1)) ^ (u & 3));
    unsigned aER = L_E + q * 1024 + (ro ^ (q << 6)), aER0 = q == 1 ? aER + 1024 : aER;
    unsigned aPW = L_PART + u * 32 + (lane >> 3) * 4, aPR = L_PART + lane * 4, aST = lane * 16;
    PIN(AI00); PIN(AI01); PIN(AI10); PIN(AI11); PIN(xc0); PIN(xc1); PIN(tc); PIN(ts); PIN(GR); PIN(GI); PIN(Me0); PIN(Mo0); PIN(Me1); PIN(Mo1);
    PIN(aLD); PIN(aEWe); PIN(aEWo); PIN(aER); PIN(aER0); PIN(aPW); PIN(aPR); PIN(aST);
    DRAIN();
    waitall(Y, 6);
    __builtin_amdgcn_s_sleep(PRE2);
    STAMP(15);
    u4 z;
#pragma unroll 1
    for (unsigned s = 0; s < SPIN_CAP; ++s) {
      FENCE();
#pragma unroll
      for (int i = 0; i < 4; ++i) z[i] = ldw(rws, aLD, WS_X2 + b * 32 + (u * 32 + 2 * i) * 8192);
      if (!anysent(mx(0u, z))) break;
    }
    STAMP(5);
    const f4 gr = mfma_b(z, GR, Z), gi = mfma_b(z, GI, Z);
    f4 r = mfma_b(z, IR, Z), im = mfma_b(z, II, Z);
    const bool sel = w == 0 && g == 0;
    r[0] = sel ? gr[0] : r[0]; im[0] = sel ? gi[0] : im[0];
    cmul(r, im, tc, ts);
    const u4 U = zipb(r, im);
    const f4 e0 = mfma_b(U, Me0, Z), o0 = mfma_b(U, Mo0, Z), e1 = mfma_b(U, Me1, Z), o1 = mfma_b(U, Mo1, Z);
    const u4 pe = catb(e0, e1), po = catb(o0, o1);
    *lp<u2>(aEWe) = pe.xy; *lp<u2>(aEWo) = po.xy;
    *lp<u2>(aEWe + 512) = pe.zw; *lp<u2>(aEWo + 512) = po.zw;
    sig(Y, 7);
    u4 k0, k1;
    WAIT_READ(7, k0 = cat(trrd(aER0), trrd(aER + 4096)); k1 = cat(trrd(aER + 8192), trrd(aER + 12288)); PIN(k0); PIN(k1));
    const f4 h0 = mfma_b(AI01, k1, mfma_b(AI00, k0, xc0)), h1 = mfma_b(AI11, k1, mfma_b(AI10, k0, xc1));
    const f4 q0 = h0 * h0, q1 = h1 * h1;
    const f2 a0 = swap32(q0[0], q1[0]), a1 = swap32(q0[1], q1[1]), a2 = swap32(q0[2], q1[2]), a3 = swap32(q0[3], q1[3]);
    const f2 b0 = swap16(a0.x + a0.y, a2.x + a2.y), b1 = swap16(a1.x + a1.y, a3.x + a3.y);
    float pr;
    asm volatile("s_nop 1\n\tv_add_f32_dpp %0, %1, %1 row_ror:8 row_mask:0xf bank_mask:0x3\n\tv_add_f32_dpp %0, %2, %2 row_ror:8 row_mask:0xf bank_mask:0xc\n\t"
                 "s_nop 1\n\tv_add_f32_dpp %0, %0, %0 row_half_mirror row_mask:0xf bank_mask:0xf\n\t"
                 "s_nop 1\n\tv_add_f32_dpp %0, %0, %0 quad_perm:[2,3,0,1] row_mask:0xf bank_mask:0xf\n\t"
                 "s_nop 1\n\tv_add_f32_dpp %0, %0, %0 quad_perm:[1,0,3,2] row_mask:0xf bank_mask:0xf"
                 : "=&v"(pr) : "v"(b0.x + b0.y), "v"(b1.x + b1.y));
    *lp<float>(aPW) = pr;
    sig(Y, 8);
    float ps;
    WAIT_READ(8, ps = *lp<float>(aPR); PIN(ps));
    ps = ror_add<0x128>(ps);
    const f2 p16 = swap16(ps, ps); ps = p16.x + p16.y;
    const f2 p32 = swap32(ps, ps); ps = p32.x + p32.y;
    const int rsv = __builtin_bit_cast(int, __builtin_amdgcn_rsqf(fmaf(ps, (1.f + 1e-5f) / 512.f, 1e-10f)));
    f4 r0, r1;
#pragma unroll
    for (int i = 0; i < 4; ++i) { r0[i] = __builtin_bit_cast(float, __builtin_amdgcn_readlane(rsv, i)); r1[i] = __builtin_bit_cast(float, __builtin_amdgcn_readlane(rsv, 4 + i)); }
    stx(catb(h0 * r0, h1 * r1), rws, aST, WS_HT + u * 262144 + b * 1024);
    STAMP(6);
    FENCE();
    *lp<u4>(L_A + u * 1024 + dn * 16) = catb(h0 + m0, h1 + m1);
    FENCE();
    const unsigned ph = L_A + u * 1024 + (16 * g + 8 * (p4 >> 1) + q) * 16 + 8 * (p4 & 1);
    stx(cat(trrd(ph), trrd(ph + 64)), rws, (c & 7) * 262144 + (2 * g + (c >> 3)) * 16, WS_H + s0 * 1024 + u * 128);
  }
  const unsigned vo = g * 1024 + c * 16 + t * 512;
  if (g1) {
    u4 braw;
    if (u < 4) braw = *(const u4*)(b1 + f0 + 8 * g); else braw.xy = *(const u2*)(b1 + f0 + 32 + 4 * g);
    DRAIN();
    __builtin_amdgcn_s_sleep(PRE3);
    const unsigned hb = WS_HT + dt * 262144 + w * 65536, cano = lane * 1024 + t * 512 + 508;
    u4 B[4][2];
#pragma unroll 1
    for (unsigned s = 0; s < SPIN_CAP; ++s) {
      FENCE();
      const unsigned can = ldw(rws, cano, hb);
#pragma unroll
      for (int kk = 0; kk < 4; ++kk)
#pragma unroll
        for (int mt = 0; mt < 2; ++mt) { B[kk][mt] = ldx(rws, vo + mt * 256, hb + kk * 4096); __builtin_amdgcn_sched_barrier(0); }
      if (!anysent(can)) break;
#pragma unroll 1
      for (; s < SPIN_CAP; ++s) { FENCE(); if (!anysent(ldw(rws, cano, hb))) break; }
    }
    STAMP(7);
    f4 acc[3][2];
#pragma unroll
    for (int nt = 0; nt < 3; ++nt) { acc[nt][0] = Z; acc[nt][1] = Z; }
#pragma unroll
    for (int n = 0; n < 4; ++n) {
      if (n < 3) waitw(Y, F_PROG, 4 * n + 8);
      const unsigned slot = a1off + (n < 2 ? (4 * n + 4) * 3072 : 0);
#pragma unroll
      for (int kk = 0; kk < 4; ++kk) {
        if (__builtin_expect(anysent(mx(mx(0u, B[kk][0]), B[kk][1])), 0)) reload2(B[kk], rws, vo, hb + (4 * n + kk) * 4096);
#pragma unroll
        for (int mt = 0; mt < 2; ++mt)
#pragma unroll
          for (int nt = 0; nt < 3; ++nt) acc[nt][mt] = mfma_b(WA[kk][nt], B[kk][mt], acc[nt][mt]);
        if (n < 3) {
#pragma unroll
          for (int mt = 0; mt < 2; ++mt) B[kk][mt] = ldx(rws, vo + mt * 256, hb + (4 * n + kk + 4) * 4096);
#pragma unroll
          for (int nt = 0; nt < 3; ++nt) {
            const unsigned off = slot + kk * 3072 + (nt < 2 ? 16 * p4 + 8 * nt : 64 + 8 * p4);
            WA[kk][nt] = cat(trrd(off), trrd(off + 1536));
          }
        }
        if (n == 3 && kk == 2) waitall(Y, 9);
        __builtin_amdgcn_sched_barrier(0);
      }
      if (n == 2) sig(Y, 9);
    }
    STAMP(8);
#pragma unroll
    for (int mt = 0; mt < 2; ++mt)
#pragma unroll
      for (int nt = 0; nt < 3; ++nt) *lp<f4>(L_W + w * L_WREG + ((2 * t + mt) * 3 + nt) * 1024 + lane * 16) = acc[nt][mt];
    sig(Y, 10);
    const unsigned gbase = WS_G + (dt * 172 + (f0 >> 3)) * 1024 + (u & 3) * 256;
    const unsigned pr = L_W + ((u & 3) * 3 + (u >> 2) * 2) * 1024 + lane * 16;
    auto gelu = [](f4 v) {
      const f4 z = v * (v * v * -0.10294324f - 2.3022082f);
      f4 e = {__builtin_amdgcn_exp2f(z[0]), __builtin_amdgcn_exp2f(z[1]), __builtin_amdgcn_exp2f(z[2]), __builtin_amdgcn_exp2f(z[3])};
      e += 1.f;
      const f4 r = {__builtin_amdgcn_rcpf(e[0]), __builtin_amdgcn_rcpf(e[1]), __builtin_amdgcn_rcpf(e[2]), __builtin_amdgcn_rcpf(e[3])};
      return v * r;
    };
    const f4 bA = {blo(braw[0]), bhi(braw[0]), blo(braw[1]), bhi(braw[1])};
    if (u < 4) {
      const f4 bB = {blo(braw[2]), bhi(braw[2]), blo(braw[3]), bhi(braw[3])};
      f4 pa[4], pb[4];
      waitall(Y, 10);
#pragma unroll
      for (int v = 0; v < 4; ++v) { pa[v] = *lp<f4>(pr + v * L_WREG); pb[v] = *lp<f4>(pr + v * L_WREG + 1024); }
      sig(Y, 11);
      stx(catb(gelu(((pa[0] + bA) + pa[1]) + (pa[2] + pa[3])), gelu(((pb[0] + bB) + pb[1]) + (pb[2] + pb[3]))), rws, g * 1024 + c * 16, gbase);
    } else {
      f4 pa[4];
      waitall(Y, 10);
#pragma unroll
      for (int v = 0; v < 4; ++v) pa[v] = *lp<f4>(pr + v * L_WREG);
      sig(Y, 11);
      const f4 gv = gelu(((pa[0] + bA) + pa[1]) + (pa[2] + pa[3]));
      const u2 gb = {pkb(gv[0], gv[1]), pkb(gv[2], gv[3])};
      __builtin_amdgcn_raw_buffer_store_b64(gb, rws, (4 + (g >> 1)) * 1024 + c * 16 + (g & 1) * 8, gbase, SC1);
    }
  } else {
    sig(Y, 9); sig(Y, 11);
    __builtin_amdgcn_s_sleep(100);
  }
  STAMP(9);
  {
    const int srow = st * 64 + w * 16 + c;
    const float bias = blo(b2[srow]);
    const unsigned hvo = c * 1024 + g * 16, hso = WS_H + (st * 64 + w * 16) * 1024 + dt * 128 + t * 64;
    u4 hv = ldx(rws, hvo, hso);
    u4 WB[2][4];
    const int rho = 4 * g + q;
    const unsigned b2off = L_W + w * L_WREG + rho * 128 + 8 * p4, sw = ((rho >> 1) & 3) ^ w;
    waitw(Y, F_PROG, 20);
#pragma unroll
    for (int kk = 0; kk < 2; ++kk)
#pragma unroll
      for (int nt = 0; nt < 4; ++nt) { const unsigned off = b2off + 12288 + kk * 4096 + ((nt ^ sw) * 32); WB[kk][nt] = cat(trrd(off), trrd(off + 2048)); }
    sig(Y, 12);
    DRAIN();
#pragma unroll 1
    for (unsigned s = 0; s < SPIN_CAP && anysent(mx(0u, hv)); ++s) { FENCE(); hv = ldx(rws, hvo, hso); }
    f4 ha = {blo(hv[0]) + bias, bhi(hv[0]) + bias, blo(hv[1]) + bias, bhi(hv[1]) + bias}, hb4 = {blo(hv[2]) + bias, bhi(hv[2]) + bias, blo(hv[3]) + bias, bhi(hv[3]) + bias};
    PIN(ha); PIN(hb4);
    __builtin_amdgcn_s_sleep(PRE4);
    const unsigned gb = WS_G + (dt * 172 + w * 44) * 1024, glast = gb + (w == 3 ? 9 : 10) * 4096;
    const unsigned cano = min(lane, w == 3 ? 39 : 43) * 1024 + t * 512 + 508;
    u4 A[4][2];
#pragma unroll 1
    for (unsigned s = 0; s < SPIN_CAP; ++s) {
      FENCE();
      const unsigned can = ldw(rws, cano, gb);
#pragma unroll
      for (int kk = 0; kk < 4; ++kk)
#pragma unroll
        for (int mt = 0; mt < 2; ++mt) { A[kk][mt] = ldx(rws, vo + mt * 256, gb + kk * 4096); __builtin_amdgcn_sched_barrier(0); }
      if (!anysent(can)) break;
#pragma unroll 1
      for (; s < SPIN_CAP; ++s) { FENCE(); if (!anysent(ldw(rws, cano, gb))) break; }
    }
    STAMP(10);
    f4 acc[2][4];
#pragma unroll
    for (int nt = 0; nt < 4; ++nt) { acc[0][nt] = Z; acc[1][nt] = Z; }
#pragma unroll
    for (int ks = 0; ks < 11; ++ks) {
      const int kk = ks & 3;
      if (ks == 2) waitw(Y, F_PROG, 25);
      if (ks == 7) waitw(Y, F_PROG, 27);
      if (ks == 10) waitall(Y, 13);
      if (__builtin_expect(anysent(mx(mx(0u, A[kk][0]), A[kk][1])), 0)) reload2(A[kk], rws, vo, ks == 10 ? glast : gb + ks * 4096);
      if (ks == 10) { const unsigned km = w == 3 ? 0u : ~0u; A[kk][0] &= km; A[kk][1] &= km; }
#pragma unroll
      for (int mt = 0; mt < 2; ++mt)
#pragma unroll
        for (int nt = 0; nt < 4; ++nt) acc[mt][nt] = mfma_b(A[kk][mt], WB[ks & 1][nt], acc[mt][nt]);
      if (ks + 4 < 11) {
#pragma unroll
        for (int mt = 0; mt < 2; ++mt) A[kk][mt] = ldx(rws, vo + mt * 256, ks + 4 == 10 ? glast : gb + (ks + 4) * 4096);
      }
      if (ks + 2 < 11) {
        const int k4 = ks + 2, so = k4 < 6 ? 12288 + k4 * 4096 : (k4 < 9 ? (k4 - 6) * 4096 : 12288 + (k4 - 9) * 4096);
#pragma unroll
        for (int nt = 0; nt < 4; ++nt) { const unsigned off = b2off + so + ((nt ^ sw) * 32); WB[ks & 1][nt] = cat(trrd(off), trrd(off + 2048)); }
      }
      if (ks == 8) sig(Y, 13);
      __builtin_amdgcn_sched_barrier(0);
    }
    STAMP(11);
#pragma unroll
    for (int nt = 1; nt < 4; ++nt)
#pragma unroll
      for (int mt = 0; mt < 2; ++mt) *lp<f4>(L_W + w * L_WREG + (nt * 4 + 2 * t + mt) * 1024 + lane * 16) = acc[mt][nt];
    sig(Y, 14);
    ha += acc[0][0]; hb4 += acc[1][0];
    f4 pa[4], pb[4];
    waitall(Y, 14);
#pragma unroll
    for (int v = 1; v < 4; ++v) { const unsigned pr = L_W + (w ^ v) * L_WREG + (v * 4 + 2 * t) * 1024 + lane * 16; pa[v] = *lp<f4>(pr); pb[v] = *lp<f4>(pr + 1024); }
    *(u4*)(out + (size_t)srow * 512 + dt * 64 + t * 32 + g * 8) = catb((ha + pa[1]) + (pa[2] + pa[3]), (hb4 + pb[1]) + (pb[2] + pb[3]));
  }
  STAMP(12);
  if (u == 0 && ts1 && (TBLK < 0 || (int)blockIdx.x == TBLK)) { while (rtc() - ts0 < 6000 + 10 * (ts1 - ts0)) __builtin_amdgcn_s_sleep(2); }
}

extern "C" void kernel_launch(void* const* d_in, const int* in_sizes, int n_in, void* d_out, int out_size, void* d_ws, size_t ws_size,
                              hipStream_t stream) {
  (void)hipFuncSetAttribute((const void*)fused_kernel, hipFuncAttributeMaxDynamicSharedMemorySize, L_BYTES);
  fused_kernel<<<256, 640, L_BYTES, stream>>>((const unsigned short*)d_in[0], (const unsigned short*)d_in[1], (const unsigned short*)d_in[2],
                                              (const unsigned short*)d_in[4], (const unsigned short*)d_in[5], (const unsigned short*)d_in[6],
                                              (const unsigned short*)d_in[7], (unsigned short*)d_out, (unsigned char*)d_ws);
}
```

```cpp
#define OUTAUX 16
#include <hip/hip_runtime.h>
#include <hip/hip_bf16.h>

typedef _Float16 h8 __attribute__((ext_vector_type(8)));
typedef _Float16 h2 __attribute__((ext_vector_type(2)));
typedef __bf16 b8 __attribute__((ext_vector_type(8)));
typedef __bf16 b2 __attribute__((ext_vector_type(2)));
typedef float f2 __attribute__((ext_vector_type(2)));
typedef float f4 __attribute__((ext_vector_type(4)));
typedef short s4 __attribute__((ext_vector_type(4)));
typedef unsigned u2 __attribute__((ext_vector_type(2)));
typedef unsigned u4 __attribute__((ext_vector_type(4)));
#define LAS __attribute__((address_space(3)))
#define DEV __device__ __forceinline__
#define FENCE() asm volatile("" ::: "memory")
#define PIN(x) asm volatile("" : "+v"(x))
#define DRAIN() asm volatile("s_waitcnt vmcnt(0)" ::: "memory")

#define STAMPS 0
#define PRE1 2
#define PRE2 2
#define PRE3 2
#define PRE4 2
#define WINL 9
#define PREW1 1
#define PREW1W 6
#define PREW2 4
#define ABL_WBYTES (2048 * 257 * 2)
#define ABL_W1BYTES (2048 * 1376 * 2)
#define TBLK -1
#define CODEPF 1

#define CODE_BYTES 20480
constexpr int SC1 = 16;
constexpr unsigned SENT = 0xFFFFFFFFu, OOB = 0x7FFF0000u;
constexpr unsigned SPIN_CAP = 1u << 13;
constexpr unsigned WS_X1 = 0, WS_X2 = 2u << 20, WS_HT = 4u << 20, WS_H = 6u << 20, WS_G = 8u << 20;
constexpr unsigned WS_FLAG = 10u << 20, WS_SEED = WS_FLAG + 65536, WS_END = WS_SEED + 4096;
constexpr unsigned WS_STAMP = 16u << 20;
constexpr unsigned L_W = 0, L_WREG = 36864, L_E = 4 * L_WREG, L_A = L_E + 8192, L_BYTES = L_E + 16384;
constexpr unsigned L_FLAG = L_E + 1024, F_PROG = 192, F_HELP = 196, L_MEAN = L_FLAG + 256, L_PART = L_FLAG + 512, L_SINK = L_FLAG + 768;

struct Tabs { unsigned fwd[5][8]; unsigned inv[5][16]; };
constexpr unsigned short cs8(int n, unsigned short one, unsigned short rt) {
  n &= 7; if (n == 2 || n == 6) return 0; const unsigned short m = (n & 1) ? rt : one; return (n >= 3 && n <= 5) ? (unsigned short)(m | 0x8000) : m;
}
constexpr unsigned short comb(int w, int eo, int r, unsigned short one, unsigned short rt) { return w == 0 ? cs8(4 * eo * r, one, rt) : cs8(r * w - 2 * eo, one, rt); }
constexpr Tabs make_tabs() {
  Tabs t = {};
  for (int i = 0; i < 4; ++i) {
    for (int h = 0; h < 2; ++h) for (int j = 0; j < 4; ++j) { const int o = 4 * h + i;
      t.fwd[i][4 * h + j] = comb(o >> 1, o & 1, 2 * j, 0x3800, 0x35A8) | ((unsigned)comb(o >> 1, o & 1, 2 * j + 1, 0x3800, 0x35A8) << 16); }
    for (int h = 0; h < 2; ++h) for (int j = 0; j < 8; ++j) { const int r = 4 * h + i, w = j >> 1, tt = j & 1;
      unsigned e = comb(w, 0, r, 0x3B00, 0x3AB5), o = comb(w, 1, r, 0x3B00, 0x3AB5);
      if (w == 0) { if (tt) e = 0; else o = 0; }
      t.inv[i][8 * h + j] = e | (o << 16); }
  }
  return t;
}
__device__ const Tabs __attribute__((aligned(16))) TABS = make_tabs();

DEV f4 mfma_h(u4 a, u4 b, f4 c) { return __builtin_amdgcn_mfma_f32_16x16x32_f16(__builtin_bit_cast(h8, a), __builtin_bit_cast(h8, b), c, 0, 0, 0); }
DEV f4 mfma_b(u4 a, u4 b, f4 c) { return __builtin_amdgcn_mfma_f32_16x16x32_bf16(__builtin_bit_cast(b8, a), __builtin_bit_cast(b8, b), c, 0, 0, 0); }
DEV unsigned pkh(float a, float b) { f2 v = {a, b}; return __builtin_bit_cast(unsigned, __builtin_convertvector(v, h2)); }
DEV unsigned pkb(float a, float b) { f2 v = {a, b}; return __builtin_bit_cast(unsigned, __builtin_convertvector(v, b2)); }
DEV u4 ziph(f4 a, f4 b) { u4 r = {pkh(a[0], b[0]), pkh(a[1], b[1]), pkh(a[2], b[2]), pkh(a[3], b[3])}; return r; }
DEV u4 zipb(f4 a, f4 b) { u4 r = {pkb(a[0], b[0]), pkb(a[1], b[1]), pkb(a[2], b[2]), pkb(a[3], b[3])}; return r; }
DEV u4 catb(f4 a, f4 b) { u4 r = {pkb(a[0], a[1]), pkb(a[2], a[3]), pkb(b[0], b[1]), pkb(b[2], b[3])}; return r; }
DEV float blo(unsigned u) { return __builtin_bit_cast(float, u << 16); }
DEV float bhi(unsigned u) { return __builtin_bit_cast(float, u & 0xFFFF0000u); }
DEV f4 cos4(f4 r) { f4 o = {__builtin_amdgcn_cosf(r[0]), __builtin_amdgcn_cosf(r[1]), __builtin_amdgcn_cosf(r[2]), __builtin_amdgcn_cosf(r[3])}; return o; }
DEV f4 sin4(f4 r) { f4 o = {__builtin_amdgcn_sinf(r[0]), __builtin_amdgcn_sinf(r[1]), __builtin_amdgcn_sinf(r[2]), __builtin_amdgcn_sinf(r[3])}; return o; }
template <class T> DEV LAS T* lp(unsigned off) { return (LAS T*)(size_t)off; }
DEV u2 trrd(unsigned off) { return __builtin_bit_cast(u2, __builtin_amdgcn_ds_read_tr16_b64_v4i16(lp<s4>(off))); }
DEV u4 cat(u2 a, u2 b) { u4 r = {a.x, a.y, b.x, b.y}; return r; }
DEV unsigned mx(unsigned m, u4 v) { return max(max(max(m, v.x), v.y), max(v.z, v.w)); }
DEV bool anysent(unsigned m) { return __builtin_amdgcn_ballot_w64(m == SENT) != 0; }
DEV u4 ldx(__amdgpu_buffer_rsrc_t r, unsigned v, unsigned s) { return __builtin_amdgcn_raw_buffer_load_b128(r, v, s, SC1); }
DEV unsigned ldw(__amdgpu_buffer_rsrc_t r, unsigned v, unsigned s) { return __builtin_amdgcn_raw_buffer_load_b32(r, v, s, SC1); }
DEV void stx(u4 d, __amdgpu_buffer_rsrc_t r, unsigned v, unsigned s) {
  __builtin_amdgcn_raw_buffer_store_b128(d, r, v, s, SC1);
  asm volatile("s_nop 1" ::"v"(d) : "memory");
}
DEV void reload2(u4 (&v)[2], __amdgpu_buffer_rsrc_t r, unsigned vo, unsigned so) {
#pragma unroll 1
  for (unsigned s = 0; s < SPIN_CAP; ++s) { FENCE(); v[0] = ldx(r, vo, so); v[1] = ldx(r, vo + 256, so); if (!anysent(mx(mx(0u, v[0]), v[1]))) break; }
}
template <int C> DEV float ror_add(float v) { return v + __builtin_bit_cast(float, __builtin_amdgcn_update_dpp(0, __builtin_bit_cast(int, v), C, 0xF, 0xF, true)); }
DEV float row16_sum(float t) { t = ror_add<0x128>(t); t = ror_add<0x124>(t); t = ror_add<0x122>(t); return ror_add<0x121>(t); }
DEV f2 swap32(float a, float b) { auto r = __builtin_amdgcn_permlane32_swap(__builtin_bit_cast(unsigned, a), __builtin_bit_cast(unsigned, b), false, false); const unsigned r0 = r[0], r1 = r[1]; f2 o = {__builtin_bit_cast(float, r0), __builtin_bit_cast(float, r1)}; return o; }
DEV f2 swap16(float a, float b) { auto r = __builtin_amdgcn_permlane16_swap(__builtin_bit_cast(unsigned, a), __builtin_bit_cast(unsigned, b), false, false); const unsigned r0 = r[0], r1 = r[1]; f2 o = {__builtin_bit_cast(float, r0), __builtin_bit_cast(float, r1)}; return o; }
DEV void cmul(f4& r, f4& i, f4 c, f4 s) { const f4 t = r * c - i * s; i = r * s + i * c; r = t; }
DEV void cmulc(f4& r, f4& i, f4 c, f4 s) { const f4 t = r * c + i * s; i = i * c - r * s; r = t; }

struct Sync { unsigned base, mine, one; };
DEV void sig(const Sync& y, int slot) { FENCE(); *lp<volatile unsigned char>(y.mine + 8 * slot) = (unsigned char)y.one; FENCE(); }
DEV unsigned long long flagrd(const Sync& y, int slot) { FENCE(); const unsigned long long f = *lp<volatile unsigned long long>(y.base + 8 * slot); FENCE(); return f; }
DEV bool flagok(unsigned long long f) { FENCE(); return __builtin_amdgcn_ballot_w64(f != 0x0101010101010101ull) == 0; }
DEV void waitall(const Sync& y, int slot) { while (!flagok(flagrd(y, slot))) {} }
#define WAIT_READ(slot, ...) for (;;) { const unsigned long long f_ = flagrd(Y, slot); __VA_ARGS__; if (flagok(f_)) break; }
DEV void setw(const Sync& y, unsigned off, unsigned e) { FENCE(); *lp<volatile unsigned>(y.base + off) = e; FENCE(); }
DEV void waitw(const Sync& y, unsigned off, unsigned e) {
  for (;;) { FENCE(); const unsigned f = *lp<volatile unsigned>(y.base + off); FENCE(); if (__builtin_amdgcn_ballot_w64(f < e) == 0) break; }
}
DEV void waitall_slow(const Sync& y, int slot) { while (!flagok(flagrd(y, slot))) __builtin_amdgcn_s_sleep(1); }
DEV void dma16(__amdgpu_buffer_rsrc_t r, unsigned dst, unsigned v, unsigned s) { __builtin_amdgcn_raw_ptr_buffer_load_lds(r, lp<void>(dst), 16, v, s, 0, 0); }
DEV void touch(__amdgpu_buffer_rsrc_t r, unsigned v, unsigned s) { __builtin_amdgcn_raw_ptr_buffer_load_lds(r, lp<void>(L_SINK), 4, v, s, 0, 0); }
#define STAMP(i)

__global__ void __launch_bounds__(640, 3)
fused_kernel(const unsigned short* __restrict__ x, const unsigned short* __restrict__ w_r, const unsigned short* __restrict__ w_i,
             const unsigned short* __restrict__ w1, const unsigned short* __restrict__ b1, const unsigned short* __restrict__ w2,
             const unsigned short* __restrict__ b2, unsigned short* __restrict__ out, unsigned char* ws) {
  unsigned long long pc0; asm volatile("s_getpc_b64 %0" : "=s"(pc0)); pc0 &= ~127ull;
  asm volatile("" ::"s"(x), "s"(w_r), "s"(w_i), "s"(w1), "s"(b1), "s"(w2), "s"(b2), "s"(out), "s"(ws));
  const int tid = threadIdx.x, lane = tid & 63, u = __builtin_amdgcn_readfirstlane(tid >> 6);
  const int g = lane >> 4, c = lane & 15, q = (lane >> 2) & 3, p4 = lane & 3;
  const int xcd = blockIdx.x & 7, i32 = blockIdx.x >> 3, b = xcd * 32 + i32;
  const int s0 = (b & 0xC0) | ((b & 3) << 4) | ((b >> 2) & 15);
  const int dt = 4 * (xcd & 1) + (i32 & 3), fq = xcd >> 1;
  const int ft = (fq ? 1 + 7 * fq : 0) + (i32 >> 2);
  const bool g1 = (i32 >> 2) < (fq ? 7 : 8);
  const int f0 = min(48 * ft, 1328);
  const int st = 8 * fq + (i32 >> 2);
  STAMP(0);
  const __amdgpu_buffer_rsrc_t rws = __builtin_amdgcn_make_buffer_rsrc((void*)ws, 0, WS_END, 0x00020000);
  const u4 S4 = {SENT, SENT, SENT, SENT};
  Sync Y = {L_FLAG, L_FLAG + u, 1u};
  PIN(Y.base); PIN(Y.mine); PIN(Y.one);

  if (u >= 8) {
    if (u == 9) {
      const unsigned seed = ldw(rws, 0, WS_SEED);
      __builtin_amdgcn_s_barrier();
#pragma unroll
      for (int i = 0; i < 16; ++i) __builtin_amdgcn_raw_buffer_store_b128(S4, rws, lane * 16 + (i & 3) * 1024, (i >> 3) * WS_X2 + b * 8192 + (i & 4) * 1024, SC1);
      asm volatile("s_nop 1" ::"v"(S4) : "memory");
      const u2 tag = {seed + 1u, (seed + 1u) * 0x9E3779B1u ^ 0x5BD1E995u};
      DRAIN();
      if (lane == 0) __builtin_amdgcn_raw_buffer_store_b64(tag, rws, 0, WS_FLAG + b * 128, SC1);
      setw(Y, F_HELP, 1);
      STAMP(1);
      DRAIN();
#pragma unroll 1
      for (unsigned s = 0; s < SPIN_CAP; ++s) {
        FENCE();
        unsigned bad = 0;
#pragma unroll
        for (int i = 0; i < 4; ++i) { const u2 f = __builtin_amdgcn_raw_buffer_load_b64(rws, lane * 128, WS_FLAG + i * 8192, SC1); bad |= (f.x ^ tag.x) | (f.y ^ tag.y); }
        if (!__builtin_amdgcn_ballot_w64(bad != 0)) break;
      }
      setw(Y, F_HELP, 2);
      STAMP(2);
      if (b == 0 && lane == 0) __builtin_amdgcn_raw_buffer_store_b32(tag.x, rws, 0, WS_SEED, SC1);
      return;
    }
    __builtin_amdgcn_global_load_lds((const __attribute__((address_space(1))) void*)(pc0 + min(((i32 & 3) * 64 + lane) * 128, CODE_BYTES - 128)), lp<void>(L_SINK), 4, 0, 0);
    *lp<volatile unsigned>(L_FLAG + 4 * lane) = 0u;
    asm volatile("s_waitcnt lgkmcnt(0)" ::: "memory");
    __builtin_amdgcn_s_barrier();
    const __amdgpu_buffer_rsrc_t r1 = __builtin_amdgcn_make_buffer_rsrc((void*)w1, 0, ABL_W1BYTES, 0x00020000);
    const __amdgpu_buffer_rsrc_t r2 = __builtin_amdgcn_make_buffer_rsrc((void*)w2, 0, 2048 * 1376 * 2, 0x00020000);
    {
      const int cb0 = 96 * (fq ? 1 + 7 * fq : 0), cb1 = min(cb0 + (fq ? 672 : 768), 2752) - 4;
      const unsigned tv = ((lane >> 5) + 16 * (4 * i32 + ((lane >> 3) & 3))) * 2752 + min(cb0 + 128 * (lane & 7), cb1);
#pragma unroll
      for (int n = 0; n < 8; ++n) touch(r1, tv, 2 * n * 2752);
      asm volatile("s_waitcnt vmcnt(%0)" ::"i"(PREW1W) : "memory");
    }
    if (g1) {
      unsigned vo[3];
#pragma unroll
      for (int n = 0; n < 3; ++n) {
        const int pc = 64 * n + lane, rho = pc / 6, pcol = pc - 6 * rho;
        const int gg = (rho >> 2) & 3, r = 4 * (rho >> 4) + (rho & 3);
        vo[n] = (16 * gg + 256 * r) * 2752 + f0 * 2 + pcol * 16;
      }
#pragma unroll 1
      for (int ks = 0; ks < 16; ++ks) {
        if (ks == 12) waitall_slow(Y, 6);
        if (ks >= PREW2 && ks < PREW2 + 6) touch(r2, min(43 * i32 + 8 * (ks - PREW2) + (lane >> 3), 43 * i32 + 42) * 4096 + (lane & 7) * 128, fq * 1024);
        const int slot = ks < 12 ? ks : ks - 12;
#pragma unroll
        for (int v = 0; v < 4; ++v) {
#pragma unroll
          for (int n = 0; n < 3; ++n) {
            dma16(r1, L_W + v * L_WREG + slot * 3072 + n * 1024, vo[n], (64 * v + ks) * 2752);
            asm volatile("s_waitcnt vmcnt(%0)" ::"i"(WINL) : "memory");
          }
        }
        setw(Y, F_PROG, ks);
      }
    }
    else {
      __builtin_amdgcn_s_sleep(64);
#pragma unroll 1
      for (int n = 0; n < 6; ++n) touch(r2, min(43 * i32 + 8 * n + (lane >> 3), 43 * i32 + 42) * 4096 + (lane & 7) * 128, fq * 1024);
    }
    DRAIN();
    setw(Y, F_PROG, 16);
    STAMP(1);
    unsigned vo2[4];
#pragma unroll
    for (int n = 0; n < 4; ++n) {
      const int rho = 8 * n + (lane >> 3), pp = lane & 7;
      const int fr = 8 * ((rho >> 2) & 3) + 4 * (rho >> 4) + (rho & 3);
      vo2[n] = fr * 4096 + st * 128 + ((pp ^ (((rho >> 1) & 3) << 1)) * 16);
    }
    waitall_slow(Y, 9);
#pragma unroll 1
    for (int ks = 0; ks < 11; ++ks) {
      if (ks == 6) { setw(Y, F_PROG, 20); waitall_slow(Y, 11); }
      if (ks == 9) waitall_slow(Y, 12);
      const int so = ks < 6 ? 12288 + ks * 4096 : (ks < 9 ? (ks - 6) * 4096 : 12288 + (ks - 9) * 4096);
#pragma unroll
      for (int v = 0; v < 4; ++v) {
#pragma unroll
        for (int n = 0; n < 4; ++n) dma16(r2, L_W + v * L_WREG + so + n * 1024, vo2[n], min(11 * v + ks, 42) * (32 * 4096));
      }
      asm volatile("s_waitcnt vmcnt(32)" ::: "memory");
    }
    setw(Y, F_PROG, 25);
    DRAIN();
    setw(Y, F_PROG, 27);
    return;
  }

  const __amdgpu_buffer_rsrc_t rx = __builtin_amdgcn_make_buffer_rsrc((void*)x, 0, 2048 * 1024, 0x00020000);
  u4 xa = __builtin_amdgcn_raw_buffer_load_b128(rx, lane * 16, (s0 + 256 * u) * 1024, 0);
  FENCE();
  __builtin_amdgcn_s_barrier();
  FENCE();
  const unsigned* tabf = TABS.fwd[(c >> 2) == g ? (c & 3) : 4];
  const u4 AC0 = *(const u4*)tabf, AC1 = *(const u4*)(tabf + 4);
  const int pw = b >> 6, pt = (b >> 5) & 1, pp2 = pt ? (pw ? 8 - pw : 4) : pw, k2b = (b >> 1) & 15;
  const bool mir = k2b >= 8, sym = pw && !pt, spec = (b & 1) == 0 && (!sym || (k2b & 7) == 0);
  const bool emir = pw && (pt ? mir : !mir);
  u4 wraw; unsigned wex;
  {
    const __amdgpu_buffer_rsrc_t rw = __builtin_amdgcn_make_buffer_rsrc((void*)(u < 4 ? w_r : w_i), 0, ABL_WBYTES, 0x00020000);
    const int j = pp2 + 8 * (tid & 255), jm = (2048 - j) & 2047, kb = 8 * (b & 1) + 32 * k2b;
    wraw = __builtin_amdgcn_raw_buffer_load_b128(rw, (mir ? jm : j) * 514, (mir ? 505 - kb : kb) * 2, 0);
    const int ke = pw == 0 ? 16 * (k2b ? k2b : 16) : (pt ? (mir ? 496 - kb : kb + 16) : kb);
    wex = __builtin_amdgcn_raw_buffer_load_b16(rw, (emir ? jm : j) * 514, spec ? ke * 2 : OOB, 0);
  }
  __builtin_amdgcn_raw_buffer_store_b128(S4, rws, tid * 16, WS_HT + b * 8192, SC1);
  __builtin_amdgcn_raw_buffer_store_b128(S4, rws, tid * 16, b < 172 ? WS_G + b * 8192 : OOB, SC1);
  stx(S4, rws, tid * 16, WS_H + b * 8192);
  const int w = u >> 1, t = u & 1, pu = t ? (w ? 8 - w : 4) : w;
  const float tp = (float)(s0 * pu) * (1.f / 2048.f);
  const f4 kf = {(float)(4 * g), (float)(4 * g + 1), (float)(4 * g + 2), (float)(4 * g + 3)};
  const f4 nf = kf * (float)c;
  const f4 c16 = cos4(nf * (1.f / 16.f)), s16 = sin4(nf * (1.f / 16.f));
  const f4 c32 = cos4(nf * (1.f / 32.f)), s32 = sin4(nf * (1.f / 32.f));
  u4 FR = ziph(0.25f * c16, 0.25f * s16), FI = ziph(-0.25f * s16, 0.25f * c16);
  const float A1 = (w == 0 && t) ? 0.f : 1.f, A2 = w ? (t ? -1.f : 1.f) : 0.f, A3 = 1.f - A1, A4 = -A2;
  const f4 Z = {0.f, 0.f, 0.f, 0.f};

  {
    const f4 alt = {1.f, -1.f, 1.f, -1.f};
    const f4 cc = 0.125f * ((w && t && c == 0) ? alt : c32), ns = 0.125f * ((w == 0 && c == 0) ? alt : -s32);
    u4 Mr0 = ziph(A1 * cc, A2 * ns + A3 * cc), Mi0 = ziph(A1 * ns, A4 * cc + A3 * ns);
    const unsigned sg = (c & 1) ? 0x80008000u : 0u;
    u4 Mr1 = Mr0 ^ sg, Mi1 = Mi0 ^ sg;
    const f4 n16 = c ? nf : 16.f * kf;
    f4 ang = (t ? n16 : nf) * (1.f / 512.f) + tp;
    if (w == 0 && c == 0) ang = Z;
    f4 tc = cos4(ang), ts = sin4(ang);
    u4 GR = {}, GI = {};
    if (w == 0) {
      const f4 ps = n16 * (1.f / 32.f) + tp, gc = 0.25f * cos4(ps), gs = -0.25f * sin4(ps);
      GR = (c & 1) ? ziph(Z, gc) : ziph(gc, Z); GI = (c & 1) ? ziph(Z, gs) : ziph(gs, Z);
    }
    unsigned aXW = L_A + u * 1024 + ((16 * lane) ^ ((u & 3) << 6));
    unsigned aXR = L_A + q * 1024 + ((128 * u + 32 * g + 8 * p4) ^ (q << 6));
    unsigned aEW = L_A + (c >> 3) * 1024 + 128 * u + (c & 7) * 16 + 4 * g;
    unsigned aEW2 = aEW ^ 128;
    unsigned aER = L_A + (2 * w + (c >> 3)) * 1024 + ((128 * g) ^ ((w & 1) << 7)) + (c & 7) * 16;
    unsigned aST = (c * 4 + g) * 16;
    PIN(Mr0); PIN(Mi0); PIN(Mr1); PIN(Mi1); PIN(tc); PIN(ts); PIN(GR); PIN(GI); PIN(FR); PIN(FI);
    PIN(aXW); PIN(aXR); PIN(aEW); PIN(aEW2); PIN(aER); PIN(aST);
    STAMP(13);
    PIN(xa);
    const f4 va = {blo(xa[0]), bhi(xa[0]), blo(xa[1]), bhi(xa[1])}, vb = {blo(xa[2]), bhi(xa[2]), blo(xa[3]), bhi(xa[3])};
    const f4 s4v = va + vb, q4v = va * va + vb * vb;
    const f2 sq = swap32((s4v[0] + s4v[2]) + (s4v[1] + s4v[3]), (q4v[0] + q4v[2]) + (q4v[1] + q4v[3]));
    float tt = sq.x + sq.y;
    const f2 t16 = swap16(tt, tt);
    tt = row16_sum(t16.x + t16.y);
    const f2 tot = swap32(tt, tt);
    const float mean = tot.x * (1.f / 512.f);
    const float rs = __builtin_amdgcn_rsqf(fmaxf(fmaf(-mean, mean, tot.y * (1.f / 512.f)), 0.f) + 1e-5f), mr = -mean * rs;
    const f4 na = va * rs + mr, nb = vb * rs + mr;
    const u4 xn = {pkh(na[0], na[1]), pkh(na[2], na[3]), pkh(nb[0], nb[1]), pkh(nb[2], nb[3])};
    *lp<u4>(aXW) = xn;
    sig(Y, 1);
    u4 bf;
    WAIT_READ(1, bf = cat(trrd(aXR), trrd(aXR + 4096)); PIN(bf));
    const f4 d0 = mfma_h(AC0, bf, Z), d1 = mfma_h(AC1, bf, Z);
    *lp<unsigned>(aEW) = pkh(d0[0], d0[1]);
    *lp<unsigned>(aEW2 + 2048) = pkh(d0[2], d0[3]);
    *lp<unsigned>(aEW + 4096) = pkh(d1[0], d1[1]);
    *lp<unsigned>(aEW2 + 6144) = pkh(d1[2], d1[3]);
    sig(Y, 2);
    const unsigned hl = *lp<volatile unsigned>(Y.base + F_HELP);
    u4 e0, e1;
    WAIT_READ(2, e0 = *lp<u4>(aER); e1 = *lp<u4>(aER + 512); PIN(e0); PIN(e1));
    f4 zr = mfma_h(e1, Mr1, mfma_h(e0, Mr0, Z)), zi = mfma_h(e1, Mi1, mfma_h(e0, Mi0, Z));
    cmulc(zr, zi, tc, ts);
    const u4 zp = ziph(zr, zi);
    const f4 gr = mfma_h(zp, GR, Z), gi = mfma_h(zp, GI, Z);
    f4 yr = mfma_h(zp, FR, Z), yi = mfma_h(zp, FI, Z);
    const bool sel = w == 0 && g == 0;
    yr[0] = sel ? gr[0] : yr[0]; yi[0] = sel ? gi[0] : yi[0];
    u4 ov = ziph(yr, yi);
    PIN(ov);
    if (__builtin_expect(__builtin_amdgcn_readfirstlane(hl) == 0, 0)) waitw(Y, F_HELP, 1);
    stx(ov, rws, aST, WS_X1 + b * 8192 + u * 1024);
    sig(Y, 3);
    *lp<float>(L_MEAN + 4 * u) = mean;
  }
  STAMP(2);
  u4 IR = zipb(c16, -s16), II = zipb(s16, c16);
  const int dn = 32 * (g >> 1) + 8 * (c >> 2) + 4 * (g & 1) + (c & 3);
  unsigned short xr[8];
#pragma unroll
  for (int r = 0; r < 8; ++r) xr[r] = __builtin_amdgcn_raw_buffer_load_b16(rx, dn * 2, (s0 + 256 * r) * 1024 + 128 * u, 0);
  {
    f4 tc = cos4(nf * (1.f / 256.f)), ts = sin4(nf * (1.f / 256.f));
    f4 wre, wim;
    if (spec) {
      if (u >= 4 && emir != mir) wex ^= 0x8000u;
      if (sym) wex = pkb(0.5f * (blo(wex) + (mir ? bhi(wraw[3]) : blo(wraw[0]))), 0.f);
      if (mir) wraw[3] = (wraw[3] & 0xFFFFu) | (wex << 16); else wraw[0] = (wraw[0] & 0xFFFF0000u) | wex;
    }
    waitall(Y, 3);
    *lp<u4>(L_A + tid * 16) = wraw;
    sig(Y, 15); waitall(Y, 15);
    {
      const unsigned pr = L_A + c * 16 + g * 1024 + 2 * (mir ? 7 - u : u);
#pragma unroll
      for (int i = 0; i < 4; ++i) { wre[i] = blo(*lp<unsigned short>(pr + i * 256)); wim[i] = blo(*lp<unsigned short>(pr + i * 256 + 4096)); }
      if (mir) wim = -wim;
    }
    sig(Y, 16);
    unsigned aLD = (tid >> 1) * 8192 + (tid & 1) * 16;
    unsigned aPW = L_A + (tid & 1) * 4096 + (((tid >> 1) * 4) ^ ((tid & 1) << 6));
    unsigned aPR = L_A + u * 1024 + ((16 * lane) ^ ((u >> 2) << 6));
    unsigned aST = tid * 16;
    PIN(tc); PIN(ts); PIN(wre); PIN(wim); PIN(IR); PIN(II); PIN(aLD); PIN(aPW); PIN(aPR); PIN(aST);
    DRAIN();
    waitall(Y, 16);
    waitw(Y, F_HELP, 2);
    __builtin_amdgcn_s_sleep(PRE1);
    STAMP(14);
    u4 v;
#pragma unroll 1
    for (unsigned s = 0; s < SPIN_CAP; ++s) { FENCE(); v = ldx(rws, aLD, WS_X1 + b * 32); if (!anysent(mx(0u, v))) break; }
    STAMP(3);
#pragma unroll
    for (int i = 0; i < 4; ++i) *lp<unsigned>(aPW + i * 1024) = v[i];
    sig(Y, 4);
    u4 a;
    WAIT_READ(4, a = *lp<u4>(aPR); PIN(a));
    f4 r = mfma_h(a, FR, Z), im = mfma_h(a, FI, Z);
    cmulc(r, im, tc, ts);
    u4 z = ziph(r, im);
    r = mfma_h(FR, z, Z); im = mfma_h(FI, z, Z);
    cmul(r, im, wre, wim);
    z = zipb(r, im);
    r = mfma_b(z, IR, Z); im = mfma_b(z, II, Z);
    cmul(r, im, tc, ts);
    z = zipb(r, im);
    r = mfma_b(IR, z, Z); im = mfma_b(II, z, Z);
    *lp<u4>(aPR) = zipb(r, im);
    sig(Y, 5);
    u4 ov;
    WAIT_READ(5, ov[0] = *lp<unsigned>(aPW); ov[1] = *lp<unsigned>(aPW + 1024); ov[2] = *lp<unsigned>(aPW + 2048); ov[3] = *lp<unsigned>(aPW + 3072); PIN(ov));
    stx(ov, rws, aST, WS_X2 + b * 8192);
  }
  STAMP(4);
  u4 WA[4][3];
  const unsigned a1off = L_W + w * L_WREG + (4 * g + q) * 96;
  if (g1) {
    waitw(Y, F_PROG, 4);
#pragma unroll
    for (int kk = 0; kk < 4; ++kk)
#pragma unroll
      for (int nt = 0; nt < 3; ++nt) {
        const unsigned off = a1off + kk * 3072 + (nt < 2 ? 16 * p4 + 8 * nt : 64 + 8 * p4);
        WA[kk][nt] = cat(trrd(off), trrd(off + 1536));
      }
  }
  sig(Y, 6);
  {
    const unsigned* tabi = TABS.inv[(c >> 2) == g ? (c & 3) : 4];
    u4 AI00 = *(const u4*)tabi, AI01 = *(const u4*)(tabi + 4), AI10 = *(const u4*)(tabi + 8), AI11 = *(const u4*)(tabi + 12);
    const f4 m0 = *lp<f4>(L_MEAN), m1 = *lp<f4>(L_MEAN + 16);
    f4 xc0 = {blo(xr[0]), blo(xr[1]), blo(xr[2]), blo(xr[3])}, xc1 = {blo(xr[4]), blo(xr[5]), blo(xr[6]), blo(xr[7])};
    xc0 -= m0; xc1 -= m1;
    f4 n16 = nf; if (g == 0) n16[0] = 16.f * (float)c;
    f4 ang = (t ? n16 : nf) * (1.f / 512.f) + tp;
    if (w == 0 && g == 0) ang[0] = 0.f;
    f4 tc = cos4(ang), ts = sin4(ang);
    u4 GR = {}, GI = {};
    if (w == 0) {
      const f4 ps = n16 * (1.f / 32.f) + tp; f4 gc = cos4(ps), gs = -sin4(ps);
      if (g == 0) { gc[0] *= 0.5f; gs[0] *= 0.5f; }
      const u4 gg = zipb(gc, gs);
      GR[0] = gg[0]; GR[2] = gg[2]; GI[1] = gg[1]; GI[3] = gg[3];
    }
    f4 cc = c32, ns = -s32;
    { const float alt = (c & 1) ? -1.f : 1.f; if (g == 0) { if (w && t) cc[0] = alt; if (w == 0) ns[0] = alt; } }
    u4 Me0 = zipb(A1 * cc, A1 * ns), Mo0 = zipb(A2 * ns + A3 * cc, A4 * cc + A3 * ns);
    const u4 SG = {0u, 0x80008000u, 0u, 0x80008000u};
    u4 Me1 = Me0 ^ SG, Mo1 = Mo0 ^ SG;
    unsigned aLD = (8 * g + (c >> 3)) * 8192 + (c & 7) * 4;
    const int re_ = 2 * u, ro_ = u ? 2 * u + 1 : 2;
    const unsigned wo = 32 * c + 8 * (g ^ (c >> 2));
    unsigned aEWe = L_E + re_ * 1024 + (wo ^ ((re_ & 3) << 6)), aEWo = L_E + ro_ * 1024 + (wo ^ ((ro_ & 3) << 6));
    const unsigned ro = 128 * u + 64 * (g >> 1) + 32 * (p4 >> 1) + 8 * ((2 * (p4 & 1) + (g & 1)) ^ (u & 3));
    unsigned aER = L_E + q * 1024 + (ro ^ (q << 6)), aER0 = q == 1 ? aER + 1024 : aER;
    unsigned aPW = L_PART + u * 32 + (lane >> 3) * 4, aPR = L_PART + lane * 4, aST = lane * 16;
    PIN(AI00); PIN(AI01); PIN(AI10); PIN(AI11); PIN(xc0); PIN(xc1); PIN(tc); PIN(ts); PIN(GR); PIN(GI); PIN(Me0); PIN(Mo0); PIN(Me1); PIN(Mo1);
    PIN(aLD); PIN(aEWe); PIN(aEWo); PIN(aER); PIN(aER0); PIN(aPW); PIN(aPR); PIN(aST);
    DRAIN();
    waitall(Y, 6);
    __builtin_amdgcn_s_sleep(PRE2);
    STAMP(15);
    u4 z;
#pragma unroll 1
    for (unsigned s = 0; s < SPIN_CAP; ++s) {
      FENCE();
#pragma unroll
      for (int i = 0; i < 4; ++i) z[i] = ldw(rws, aLD, WS_X2 + b * 32 + (u * 32 + 2 * i) * 8192);
      if (!anysent(mx(0u, z))) break;
    }
    STAMP(5);
    const f4 gr = mfma_b(z, GR, Z), gi = mfma_b(z, GI, Z);
    f4 r = mfma_b(z, IR, Z), im = mfma_b(z, II, Z);
    const bool sel = w == 0 && g == 0;
    r[0] = sel ? gr[0] : r[0]; im[0] = sel ? gi[0] : im[0];
    cmul(r, im, tc, ts);
    const u4 U = zipb(r, im);
    const f4 e0 = mfma_b(U, Me0, Z), o0 = mfma_b(U, Mo0, Z), e1 = mfma_b(U, Me1, Z), o1 = mfma_b(U, Mo1, Z);
    const u4 pe = catb(e0, e1), po = catb(o0, o1);
    *lp<u2>(aEWe) = pe.xy; *lp<u2>(aEWo) = po.xy;
    *lp<u2>(aEWe + 512) = pe.zw; *lp<u2>(aEWo + 512) = po.zw;
    sig(Y, 7);
    u4 k0, k1;
    WAIT_READ(7, k0 = cat(trrd(aER0), trrd(aER + 4096)); k1 = cat(trrd(aER + 8192), trrd(aER + 12288)); PIN(k0); PIN(k1));
    const f4 h0 = mfma_b(AI01, k1, mfma_b(AI00, k0, xc0)), h1 = mfma_b(AI11, k1, mfma_b(AI10, k0, xc1));
    const f4 q0 = h0 * h0, q1 = h1 * h1;
    const f2 a0 = swap32(q0[0], q1[0]), a1 = swap32(q0[1], q1[1]), a2 = swap32(q0[2], q1[2]), a3 = swap32(q0[3], q1[3]);
    const f2 b0 = swap16(a0.x + a0.y, a2.x + a2.y), b1 = swap16(a1.x + a1.y, a3.x + a3.y);
    float pr;
    asm volatile("s_nop 1\n\tv_add_f32_dpp %0, %1, %1 row_ror:8 row_mask:0xf bank_mask:0x3\n\tv_add_f32_dpp %0, %2, %2 row_ror:8 row_mask:0xf bank_mask:0xc\n\t"
                 "s_nop 1\n\tv_add_f32_dpp %0, %0, %0 row_half_mirror row_mask:0xf bank_mask:0xf\n\t"
                 "s_nop 1\n\tv_add_f32_dpp %0, %0, %0 quad_perm:[2,3,0,1] row_mask:0xf bank_mask:0xf\n\t"
                 "s_nop 1\n\tv_add_f32_dpp %0, %0, %0 quad_perm:[1,0,3,2] row_mask:0xf bank_mask:0xf"
                 : "=&v"(pr) : "v"(b0.x + b0.y), "v"(b1.x + b1.y));
    *lp<float>(aPW) = pr;
    sig(Y, 8);
    float ps;
    WAIT_READ(8, ps = *lp<float>(aPR); PIN(ps));
    ps = ror_add<0x128>(ps);
    const f2 p16 = swap16(ps, ps); ps = p16.x + p16.y;
    const f2 p32 = swap32(ps, ps); ps = p32.x + p32.y;
    const int rsv = __builtin_bit_cast(int, __builtin_amdgcn_rsqf(fmaf(ps, (1.f + 1e-5f) / 512.f, 1e-10f)));
    f4 r0, r1;
#pragma unroll
    for (int i = 0; i < 4; ++i) { r0[i] = __builtin_bit_cast(float, __builtin_amdgcn_readlane(rsv, i)); r1[i] = __builtin_bit_cast(float, __builtin_amdgcn_readlane(rsv, 4 + i)); }
    stx(catb(h0 * r0, h1 * r1), rws, aST, WS_HT + u * 262144 + b * 1024);
    STAMP(6);
    FENCE();
    *lp<u4>(L_A + u * 1024 + dn * 16) = catb(h0 + m0, h1 + m1);
    FENCE();
    const unsigned ph = L_A + u * 1024 + (16 * g + 8 * (p4 >> 1) + q) * 16 + 8 * (p4 & 1);
    stx(cat(trrd(ph), trrd(ph + 64)), rws, (c & 7) * 262144 + (2 * g + (c >> 3)) * 16, WS_H + s0 * 1024 + u * 128);
  }
  const unsigned vo = g * 1024 + c * 16 + t * 512;
  if (g1) {
    u4 braw;
    if (u < 4) braw = *(const u4*)(b1 + f0 + 8 * g); else braw.xy = *(const u2*)(b1 + f0 + 32 + 4 * g);
    DRAIN();
    __builtin_amdgcn_s_sleep(PRE3);
    const unsigned hb = WS_HT + dt * 262144 + w * 65536, cano = lane * 1024 + t * 512 + 508;
    u4 B[4][2];
#pragma unroll 1
    for (unsigned s = 0; s < SPIN_CAP; ++s) {
      FENCE();
      const unsigned can = ldw(rws, cano, hb);
#pragma unroll
      for (int kk = 0; kk < 4; ++kk)
#pragma unroll
        for (int mt = 0; mt < 2; ++mt) { B[kk][mt] = ldx(rws, vo + mt * 256, hb + kk * 4096); __builtin_amdgcn_sched_barrier(0); }
      if (!anysent(can)) break;
#pragma unroll 1
      for (; s < SPIN_CAP; ++s) { FENCE(); if (!anysent(ldw(rws, cano, hb))) break; }
    }
    STAMP(7);
    f4 acc[3][2];
#pragma unroll
    for (int nt = 0; nt < 3; ++nt) { acc[nt][0] = Z; acc[nt][1] = Z; }
#pragma unroll
    for (int n = 0; n < 4; ++n) {
      if (n < 3) waitw(Y, F_PROG, 4 * n + 8);
      const unsigned slot = a1off + (n < 2 ? (4 * n + 4) * 3072 : 0);
#pragma unroll
      for (int kk = 0; kk < 4; ++kk) {
        if (__builtin_expect(anysent(mx(mx(0u, B[kk][0]), B[kk][1])), 0)) reload2(B[kk], rws, vo, hb + (4 * n + kk) * 4096);
#pragma unroll
        for (int mt = 0; mt < 2; ++mt)
#pragma unroll
          for (int nt = 0; nt < 3; ++nt) acc[nt][mt] = mfma_b(WA[kk][nt], B[kk][mt], acc[nt][mt]);
        if (n < 3) {
#pragma unroll
          for (int mt = 0; mt < 2; ++mt) B[kk][mt] = ldx(rws, vo + mt * 256, hb + (4 * n + kk + 4) * 4096);
#pragma unroll
          for (int nt = 0; nt < 3; ++nt) {
            const unsigned off = slot + kk * 3072 + (nt < 2 ? 16 * p4 + 8 * nt : 64 + 8 * p4);
            WA[kk][nt] = cat(trrd(off), trrd(off + 1536));
          }
        }
        if (n == 3 && kk == 2) waitall(Y, 9);
        __builtin_amdgcn_sched_barrier(0);
      }
      if (n == 2) sig(Y, 9);
    }
    STAMP(8);
#pragma unroll
    for (int mt = 0; mt < 2; ++mt)
#pragma unroll
      for (int nt = 0; nt < 3; ++nt) *lp<f4>(L_W + w * L_WREG + ((2 * t + mt) * 3 + nt) * 1024 + lane * 16) = acc[nt][mt];
    sig(Y, 10);
    const unsigned gbase = WS_G + (dt * 172 + (f0 >> 3)) * 1024 + (u & 3) * 256;
    const unsigned pr = L_W + ((u & 3) * 3 + (u >> 2) * 2) * 1024 + lane * 16;
    auto gelu = [](f4 v) {
      const f4 z = v * (v * v * -0.10294324f - 2.3022082f);
      f4 e = {__builtin_amdgcn_exp2f(z[0]), __builtin_amdgcn_exp2f(z[1]), __builtin_amdgcn_exp2f(z[2]), __builtin_amdgcn_exp2f(z[3])};
      e += 1.f;
      const f4 r = {__builtin_amdgcn_rcpf(e[0]), __builtin_amdgcn_rcpf(e[1]), __builtin_amdgcn_rcpf(e[2]), __builtin_amdgcn_rcpf(e[3])};
      return v * r;
    };
    const f4 bA = {blo(braw[0]), bhi(braw[0]), blo(braw[1]), bhi(braw[1])};
    if (u < 4) {
      const f4 bB = {blo(braw[2]), bhi(braw[2]), blo(braw[3]), bhi(braw[3])};
      f4 pa[4], pb[4];
      waitall(Y, 10);
#pragma unroll
      for (int v = 0; v < 4; ++v) { pa[v] = *lp<f4>(pr + v * L_WREG); pb[v] = *lp<f4>(pr + v * L_WREG + 1024); }
      sig(Y, 11);
      stx(catb(gelu(((pa[0] + bA) + pa[1]) + (pa[2] + pa[3])), gelu(((pb[0] + bB) + pb[1]) + (pb[2] + pb[3]))), rws, g * 1024 + c * 16, gbase);
    } else {
      f4 pa[4];
      waitall(Y, 10);
#pragma unroll
      for (int v = 0; v < 4; ++v) pa[v] = *lp<f4>(pr + v * L_WREG);
      sig(Y, 11);
      const f4 gv = gelu(((pa[0] + bA) + pa[1]) + (pa[2] + pa[3]));
      const u2 gb = {pkb(gv[0], gv[1]), pkb(gv[2], gv[3])};
      __builtin_amdgcn_raw_buffer_store_b64(gb, rws, (4 + (g >> 1)) * 1024 + c * 16 + (g & 1) * 8, gbase, SC1);
    }
  } else {
    sig(Y, 9); sig(Y, 11);
    __builtin_amdgcn_s_sleep(100);
  }
  STAMP(9);
  {
    const int srow = st * 64 + w * 16 + c;
    const float bias = blo(b2[srow]);
    const unsigned hvo = c * 1024 + g * 16, hso = WS_H + (st * 64 + w * 16) * 1024 + dt * 128 + t * 64;
    u4 hv = ldx(rws, hvo, hso);
    u4 WB[2][4];
    const int rho = 4 * g + q;
    const unsigned b2off = L_W + w * L_WREG + rho * 128 + 8 * p4, sw = ((rho >> 1) & 3) ^ w;
    waitw(Y, F_PROG, 20);
#pragma unroll
    for (int kk = 0; kk < 2; ++kk)
#pragma unroll
      for (int nt = 0; nt < 4; ++nt) { const unsigned off = b2off + 12288 + kk * 4096 + ((nt ^ sw) * 32); WB[kk][nt] = cat(trrd(off), trrd(off + 2048)); }
    sig(Y, 12);
    DRAIN();
#pragma unroll 1
    for (unsigned s = 0; s < SPIN_CAP && anysent(mx(0u, hv)); ++s) { FENCE(); hv = ldx(rws, hvo, hso); }
    f4 ha = {blo(hv[0]) + bias, bhi(hv[0]) + bias, blo(hv[1]) + bias, bhi(hv[1]) + bias}, hb4 = {blo(hv[2]) + bias, bhi(hv[2]) + bias, blo(hv[3]) + bias, bhi(hv[3]) + bias};
    PIN(ha); PIN(hb4);
    __builtin_amdgcn_s_sleep(PRE4);
    const unsigned gb = WS_G + (dt * 172 + w * 44) * 1024, glast = gb + (w == 3 ? 9 : 10) * 4096;
    const unsigned cano = min(lane, w == 3 ? 39 : 43) * 1024 + t * 512 + 508;
    u4 A[4][2];
#pragma unroll 1
    for (unsigned s = 0; s < SPIN_CAP; ++s) {
      FENCE();
      const unsigned can = ldw(rws, cano, gb);
#pragma unroll
      for (int kk = 0; kk < 4; ++kk)
#pragma unroll
        for (int mt = 0; mt < 2; ++mt) { A[kk][mt] = ldx(rws, vo + mt * 256, gb + kk * 4096); __builtin_amdgcn_sched_barrier(0); }
      if (!anysent(can)) break;
#pragma unroll 1
      for (; s < SPIN_CAP; ++s) { FENCE(); if (!anysent(ldw(rws, cano, gb))) break; }
    }
    STAMP(10);
    f4 acc[2][4];
#pragma unroll
    for (int nt = 0; nt < 4; ++nt) { acc[0][nt] = Z; acc[1][nt] = Z; }
#pragma unroll
    for (int ks = 0; ks < 11; ++ks) {
      const int kk = ks & 3;
      if (ks == 2) waitw(Y, F_PROG, 25);
      if (ks == 7) waitw(Y, F_PROG, 27);
      if (ks == 10) waitall(Y, 13);
      if (__builtin_expect(anysent(mx(mx(0u, A[kk][0]), A[kk][1])), 0)) reload2(A[kk], rws, vo, ks == 10 ? glast : gb + ks * 4096);
      if (ks == 10) { const unsigned km = w == 3 ? 0u : ~0u; A[kk][0] &= km; A[kk][1] &= km; }
#pragma unroll
      for (int mt = 0; mt < 2; ++mt)
#pragma unroll
        for (int nt = 0; nt < 4; ++nt) acc[mt][nt] = mfma_b(A[kk][mt], WB[ks & 1][nt], acc[mt][nt]);
      if (ks + 4 < 11) {
#pragma unroll
        for (int mt = 0; mt < 2; ++mt) A[kk][mt] = ldx(rws, vo + mt * 256, ks + 4 == 10 ? glast : gb + (ks + 4) * 4096);
      }
      if (ks + 2 < 11) {
        const int k4 = ks + 2, so = k4 < 6 ? 12288 + k4 * 4096 : (k4 < 9 ? (k4 - 6) * 4096 : 12288 + (k4 - 9) * 4096);
#pragma unroll
        for (int nt = 0; nt < 4; ++nt) { const unsigned off = b2off + so + ((nt ^ sw) * 32); WB[ks & 1][nt] = cat(trrd(off), trrd(off + 2048)); }
      }
      if (ks == 8) sig(Y, 13);
      __builtin_amdgcn_sched_barrier(0);
    }
    STAMP(11);
#pragma unroll
    for (int nt = 1; nt < 4; ++nt)
#pragma unroll
      for (int mt = 0; mt < 2; ++mt) *lp<f4>(L_W + w * L_WREG + (nt * 4 + 2 * t + mt) * 1024 + lane * 16) = acc[mt][nt];
    sig(Y, 14);
    ha += acc[0][0]; hb4 += acc[1][0];
    f4 pa[4], pb[4];
    waitall(Y, 14);
#pragma unroll
    for (int v = 1; v < 4; ++v) { const unsigned pr = L_W + (w ^ v) * L_WREG + (v * 4 + 2 * t) * 1024 + lane * 16; pa[v] = *lp<f4>(pr); pb[v] = *lp<f4>(pr + 1024); }
    __builtin_amdgcn_raw_buffer_store_b128(catb((ha + pa[1]) + (pa[2] + pa[3]), (hb4 + pb[1]) + (pb[2] + pb[3])), __builtin_amdgcn_make_buffer_rsrc((void*)out, 0, 2048 * 1024, 0x00020000), srow * 1024 + dt * 128 + t * 64 + g * 16, 0, OUTAUX);
  }
}

extern "C" void kernel_launch(void* const* d_in, const int* in_sizes, int n_in, void* d_out, int out_size, void* d_ws, size_t ws_size,
                              hipStream_t stream) {
  (void)hipFuncSetAttribute((const void*)fused_kernel, hipFuncAttributeMaxDynamicSharedMemorySize, L_BYTES);
  fused_kernel<<<256, 640, L_BYTES, stream>>>((const unsigned short*)d_in[0], (const unsigned short*)d_in[1], (const unsigned short*)d_in[2],
                                              (const unsigned short*)d_in[4], (const unsigned short*)d_in[5], (const unsigned short*)d_in[6],
                                              (const unsigned short*)d_in[7], (unsigned short*)d_out, (unsigned char*)d_ws);
}
```

```cpp
#define TSTAMP 0
#define TFIRST 2
#define XK 5
#define XKB 1
#include <hip/hip_runtime.h>
#include <hip/hip_bf16.h>

typedef _Float16 h8 __attribute__((ext_vector_type(8)));
typedef _Float16 h2 __attribute__((ext_vector_type(2)));
typedef __bf16 b8 __attribute__((ext_vector_type(8)));
typedef __bf16 b2 __attribute__((ext_vector_type(2)));
typedef float f2 __attribute__((ext_vector_type(2)));
typedef float f4 __attribute__((ext_vector_type(4)));
typedef short s4 __attribute__((ext_vector_type(4)));
typedef unsigned u2 __attribute__((ext_vector_type(2)));
typedef unsigned u4 __attribute__((ext_vector_type(4)));
#define LAS __attribute__((address_space(3)))
#define DEV __device__ __forceinline__
#define FENCE() asm volatile("" ::: "memory")
#define PIN(x) asm volatile("" : "+v"(x))
#define DRAIN() asm volatile("s_waitcnt vmcnt(0)" ::: "memory")

#define STAMPS 0
#define PRE1 2
#define PRE2 2
#define PRE3 2
#define PRE4 2
#define WINL 9
#define PREW1 1
#define PREW1W 6
#define PREW2 4
#define ABL_WBYTES (2048 * 257 * 2)
#define ABL_W1BYTES (2048 * 1376 * 2)
#define TBLK -1
#define CODEPF 1

#define CODE_BYTES 20480
constexpr int SC1 = 16;
constexpr unsigned SENT = 0xFFFFFFFFu, OOB = 0x7FFF0000u;
constexpr unsigned SPIN_CAP = 1u << 13;
constexpr unsigned WS_X1 = 0, WS_X2 = 2u << 20, WS_HT = 4u << 20, WS_H = 6u << 20, WS_G = 8u << 20;
constexpr unsigned WS_FLAG = 10u << 20, WS_SEED = WS_FLAG + 65536, WS_END = WS_SEED + 4096;
constexpr unsigned WS_STAMP = 16u << 20;
constexpr unsigned L_W = 0, L_WREG = 36864, L_E = 4 * L_WREG, L_A = L_E + 8192, L_BYTES = L_E + 16384;
constexpr unsigned L_FLAG = L_E + 1024, F_PROG = 192, F_HELP = 196, L_MEAN = L_FLAG + 256, L_PART = L_FLAG + 512, L_SINK = L_FLAG + 768;

struct Tabs { unsigned fwd[5][8]; unsigned inv[5][16]; };
constexpr unsigned short cs8(int n, unsigned short one, unsigned short rt) {
  n &= 7; if (n == 2 || n == 6) return 0; const unsigned short m = (n & 1) ? rt : one; return (n >= 3 && n <= 5) ? (unsigned short)(m | 0x8000) : m;
}
constexpr unsigned short comb(int w, int eo, int r, unsigned short one, unsigned short rt) { return w == 0 ? cs8(4 * eo * r, one, rt) : cs8(r * w - 2 * eo, one, rt); }
constexpr Tabs make_tabs() {
  Tabs t = {};
  for (int i = 0; i < 4; ++i) {
    for (int h = 0; h < 2; ++h) for (int j = 0; j < 4; ++j) { const int o = 4 * h + i;
      t.fwd[i][4 * h + j] = comb(o >> 1, o & 1, 2 * j, 0x3800, 0x35A8) | ((unsigned)comb(o >> 1, o & 1, 2 * j + 1, 0x3800, 0x35A8) << 16); }
    for (int h = 0; h < 2; ++h) for (int j = 0; j < 8; ++j) { const int r = 4 * h + i, w = j >> 1, tt = j & 1;
      unsigned e = comb(w, 0, r, 0x3B00, 0x3AB5), o = comb(w, 1, r, 0x3B00, 0x3AB5);
      if (w == 0) { if (tt) e = 0; else o = 0; }
      t.inv[i][8 * h + j] = e | (o << 16); }
  }
  return t;
}
__device__ const Tabs __attribute__((aligned(16))) TABS = make_tabs();

DEV f4 mfma_h(u4 a, u4 b, f4 c) { return __builtin_amdgcn_mfma_f32_16x16x32_f16(__builtin_bit_cast(h8, a), __builtin_bit_cast(h8, b), c, 0, 0, 0); }
DEV f4 mfma_b(u4 a, u4 b, f4 c) { return __builtin_amdgcn_mfma_f32_16x16x32_bf16(__builtin_bit_cast(b8, a), __builtin_bit_cast(b8, b), c, 0, 0, 0); }
DEV unsigned pkh(float a, float b) { f2 v = {a, b}; return __builtin_bit_cast(unsigned, __builtin_convertvector(v, h2)); }
DEV unsigned pkb(float a, float b) { f2 v = {a, b}; return __builtin_bit_cast(unsigned, __builtin_convertvector(v, b2)); }
DEV u4 ziph(f4 a, f4 b) { u4 r = {pkh(a[0], b[0]), pkh(a[1], b[1]), pkh(a[2], b[2]), pkh(a[3], b[3])}; return r; }
DEV u4 zipb(f4 a, f4 b) { u4 r = {pkb(a[0], b[0]), pkb(a[1], b[1]), pkb(a[2], b[2]), pkb(a[3], b[3])}; return r; }
DEV u4 catb(f4 a, f4 b) { u4 r = {pkb(a[0], a[1]), pkb(a[2], a[3]), pkb(b[0], b[1]), pkb(b[2], b[3])}; return r; }
DEV float blo(unsigned u) { return __builtin_bit_cast(float, u << 16); }
DEV float bhi(unsigned u) { return __builtin_bit_cast(float, u & 0xFFFF0000u); }
DEV f4 cos4(f4 r) { f4 o = {__builtin_amdgcn_cosf(r[0]), __builtin_amdgcn_cosf(r[1]), __builtin_amdgcn_cosf(r[2]), __builtin_amdgcn_cosf(r[3])}; return o; }
DEV f4 sin4(f4 r) { f4 o = {__builtin_amdgcn_sinf(r[0]), __builtin_amdgcn_sinf(r[1]), __builtin_amdgcn_sinf(r[2]), __builtin_amdgcn_sinf(r[3])}; return o; }
template <class T> DEV LAS T* lp(unsigned off) { return (LAS T*)(size_t)off; }
DEV u2 trrd(unsigned off) { return __builtin_bit_cast(u2, __builtin_amdgcn_ds_read_tr16_b64_v4i16(lp<s4>(off))); }
DEV u4 cat(u2 a, u2 b) { u4 r = {a.x, a.y, b.x, b.y}; return r; }
DEV unsigned mx(unsigned m, u4 v) { return max(max(max(m, v.x), v.y), max(v.z, v.w)); }
DEV bool anysent(unsigned m) { return __builtin_amdgcn_ballot_w64(m == SENT) != 0; }
DEV u4 ldx(__amdgpu_buffer_rsrc_t r, unsigned v, unsigned s) { return __builtin_amdgcn_raw_buffer_load_b128(r, v, s, SC1); }
DEV unsigned ldw(__amdgpu_buffer_rsrc_t r, unsigned v, unsigned s) { return __builtin_amdgcn_raw_buffer_load_b32(r, v, s, SC1); }
DEV void stx(u4 d, __amdgpu_buffer_rsrc_t r, unsigned v, unsigned s) {
  __builtin_amdgcn_raw_buffer_store_b128(d, r, v, s, SC1);
  asm volatile("s_nop 1" ::"v"(d) : "memory");
}
DEV void reload2(u4 (&v)[2], __amdgpu_buffer_rsrc_t r, unsigned vo, unsigned so) {
#pragma unroll 1
  for (unsigned s = 0; s < SPIN_CAP; ++s) { FENCE(); v[0] = ldx(r, vo, so); v[1] = ldx(r, vo + 256, so); if (!anysent(mx(mx(0u, v[0]), v[1]))) break; }
}
template <int C> DEV float ror_add(float v) { return v + __builtin_bit_cast(float, __builtin_amdgcn_update_dpp(0, __builtin_bit_cast(int, v), C, 0xF, 0xF, true)); }
DEV float row16_sum(float t) { t = ror_add<0x128>(t); t = ror_add<0x124>(t); t = ror_add<0x122>(t); return ror_add<0x121>(t); }
DEV f2 swap32(float a, float b) { auto r = __builtin_amdgcn_permlane32_swap(__builtin_bit_cast(unsigned, a), __builtin_bit_cast(unsigned, b), false, false); const unsigned r0 = r[0], r1 = r[1]; f2 o = {__builtin_bit_cast(float, r0), __builtin_bit_cast(float, r1)}; return o; }
DEV f2 swap16(float a, float b) { auto r = __builtin_amdgcn_permlane16_swap(__builtin_bit_cast(unsigned, a), __builtin_bit_cast(unsigned, b), false, false); const unsigned r0 = r[0], r1 = r[1]; f2 o = {__builtin_bit_cast(float, r0), __builtin_bit_cast(float, r1)}; return o; }
DEV void cmul(f4& r, f4& i, f4 c, f4 s) { const f4 t = r * c - i * s; i = r * s + i * c; r = t; }
DEV void cmulc(f4& r, f4& i, f4 c, f4 s) { const f4 t = r * c + i * s; i = i * c - r * s; r = t; }

struct Sync { unsigned base, mine, one; };
DEV void sig(const Sync& y, int slot) { FENCE(); *lp<volatile unsigned char>(y.mine + 8 * slot) = (unsigned char)y.one; FENCE(); }
DEV unsigned long long flagrd(const Sync& y, int slot) { FENCE(); const unsigned long long f = *lp<volatile unsigned long long>(y.base + 8 * slot); FENCE(); return f; }
DEV bool flagok(unsigned long long f) { FENCE(); return __builtin_amdgcn_ballot_w64(f != 0x0101010101010101ull) == 0; }
DEV void waitall(const Sync& y, int slot) { while (!flagok(flagrd(y, slot))) {} }
#define WAIT_READ(slot, ...) for (;;) { const unsigned long long f_ = flagrd(Y, slot); __VA_ARGS__; if (flagok(f_)) break; }
DEV void setw(const Sync& y, unsigned off, unsigned e) { FENCE(); *lp<volatile unsigned>(y.base + off) = e; FENCE(); }
DEV void waitw(const Sync& y, unsigned off, unsigned e) {
  for (;;) { FENCE(); const unsigned f = *lp<volatile unsigned>(y.base + off); FENCE(); if (__builtin_amdgcn_ballot_w64(f < e) == 0) break; }
}
DEV void waitall_slow(const Sync& y, int slot) { while (!flagok(flagrd(y, slot))) __builtin_amdgcn_s_sleep(1); }
DEV void dma16(__amdgpu_buffer_rsrc_t r, unsigned dst, unsigned v, unsigned s) { __builtin_amdgcn_raw_ptr_buffer_load_lds(r, lp<void>(dst), 16, v, s, 0, 0); }
DEV void touch(__amdgpu_buffer_rsrc_t r, unsigned v, unsigned s) { __builtin_amdgcn_raw_ptr_buffer_load_lds(r, lp<void>(L_SINK), 4, v, s, 0, 0); }
DEV unsigned long long rtc() { unsigned long long t_; asm volatile("s_memrealtime %0\n\ts_waitcnt lgkmcnt(0)" : "=s"(t_) :: "memory"); return t_; }
#define STAMP(i) do { if (i == 0) ts0 = rtc(); if (i == TSTAMP) ts1 = rtc(); } while (0)

__global__ void __launch_bounds__(640, 3)
fused_kernel(const unsigned short* __restrict__ x, const unsigned short* __restrict__ w_r, const unsigned short* __restrict__ w_i,
             const unsigned short* __restrict__ w1, const unsigned short* __restrict__ b1, const unsigned short* __restrict__ w2,
             const unsigned short* __restrict__ b2, unsigned short* __restrict__ out, unsigned char* ws) {
  unsigned long long tsf; asm volatile("s_memrealtime %0" : "=s"(tsf) :: "memory"); __builtin_amdgcn_sched_barrier(0);
  unsigned long long pc0; asm volatile("s_getpc_b64 %0" : "=s"(pc0)); pc0 &= ~127ull;
  asm volatile("" ::"s"(x), "s"(w_r), "s"(w_i), "s"(w1), "s"(b1), "s"(w2), "s"(b2), "s"(out), "s"(ws));
  const int tid = threadIdx.x, lane = tid & 63, u = __builtin_amdgcn_readfirstlane(tid >> 6);
  const int g = lane >> 4, c = lane & 15, q = (lane >> 2) & 3, p4 = lane & 3;
  const int xcd = blockIdx.x & 7, i32 = blockIdx.x >> 3, b = xcd * 32 + i32;
  const int s0 = (b & 0xC0) | ((b & 3) << 4) | ((b >> 2) & 15);
  const int dt = 4 * (xcd & 1) + (i32 & 3), fq = xcd >> 1;
  const int ft = (fq ? 1 + 7 * fq : 0) + (i32 >> 2);
  const bool g1 = (i32 >> 2) < (fq ? 7 : 8);
  const int f0 = min(48 * ft, 1328);
  const int st = 8 * fq + (i32 >> 2);
  unsigned long long ts0 = 0, ts1 = 0;
  STAMP(0);
  ts0 = tsf;
  ts1 = tsf;
  const __amdgpu_buffer_rsrc_t rws = __builtin_amdgcn_make_buffer_rsrc((void*)ws, 0, WS_END, 0x00020000);
  const u4 S4 = {SENT, SENT, SENT, SENT};
  Sync Y = {L_FLAG, L_FLAG + u, 1u};
  PIN(Y.base); PIN(Y.mine); PIN(Y.one);

  if (u >= 8) {
    if (u == 9) {
      const unsigned seed = ldw(rws, 0, WS_SEED);
      __builtin_amdgcn_s_barrier();
#pragma unroll
      for (int i = 0; i < 16; ++i) __builtin_amdgcn_raw_buffer_store_b128(S4, rws, lane * 16 + (i & 3) * 1024, (i >> 3) * WS_X2 + b * 8192 + (i & 4) * 1024, SC1);
      asm volatile("s_nop 1" ::"v"(S4) : "memory");
      const u2 tag = {seed + 1u, (seed + 1u) * 0x9E3779B1u ^ 0x5BD1E995u};
      DRAIN();
      if (lane == 0) __builtin_amdgcn_raw_buffer_store_b64(tag, rws, 0, WS_FLAG + b * 128, SC1);
      setw(Y, F_HELP, 1);
      STAMP(1);
      DRAIN();
#pragma unroll 1
      for (unsigned s = 0; s < SPIN_CAP; ++s) {
        FENCE();
        unsigned bad = 0;
#pragma unroll
        for (int i = 0; i < 4; ++i) { const u2 f = __builtin_amdgcn_raw_buffer_load_b64(rws, lane * 128, WS_FLAG + i * 8192, SC1); bad |= (f.x ^ tag.x) | (f.y ^ tag.y); }
        if (!__builtin_amdgcn_ballot_w64(bad != 0)) break;
      }
      setw(Y, F_HELP, 2);
      STAMP(2);
      if (b == 0 && lane == 0) __builtin_amdgcn_raw_buffer_store_b32(tag.x, rws, 0, WS_SEED, SC1);
      return;
    }
    __builtin_amdgcn_global_load_lds((const __attribute__((address_space(1))) void*)(pc0 + min(((i32 & 3) * 64 + lane) * 128, CODE_BYTES - 128)), lp<void>(L_SINK), 4, 0, 0);
    *lp<volatile unsigned>(L_FLAG + 4 * lane) = 0u;
    asm volatile("s_waitcnt lgkmcnt(0)" ::: "memory");
    __builtin_amdgcn_s_barrier();
    const __amdgpu_buffer_rsrc_t r1 = __builtin_amdgcn_make_buffer_rsrc((void*)w1, 0, ABL_W1BYTES, 0x00020000);
    const __amdgpu_buffer_rsrc_t r2 = __builtin_amdgcn_make_buffer_rsrc((void*)w2, 0, 2048 * 1376 * 2, 0x00020000);
    {
      const int cb0 = 96 * (fq ? 1 + 7 * fq : 0), cb1 = min(cb0 + (fq ? 672 : 768), 2752) - 4;
      const unsigned tv = ((lane >> 5) + 16 * (4 * i32 + ((lane >> 3) & 3))) * 2752 + min(cb0 + 128 * (lane & 7), cb1);
#pragma unroll
      for (int n = 0; n < 8; ++n) touch(r1, tv, 2 * n * 2752);
      asm volatile("s_waitcnt vmcnt(%0)" ::"i"(PREW1W) : "memory");
    }
    if (g1) {
      unsigned vo[3];
#pragma unroll
      for (int n = 0; n < 3; ++n) {
        const int pc = 64 * n + lane, rho = pc / 6, pcol = pc - 6 * rho;
        const int gg = (rho >> 2) & 3, r = 4 * (rho >> 4) + (rho & 3);
        vo[n] = (16 * gg + 256 * r) * 2752 + f0 * 2 + pcol * 16;
      }
#pragma unroll 1
      for (int ks = 0; ks < 16; ++ks) {
        if (ks == 12) waitall_slow(Y, 6);
        if (ks >= PREW2 && ks < PREW2 + 6) touch(r2, min(43 * i32 + 8 * (ks - PREW2) + (lane >> 3), 43 * i32 + 42) * 4096 + (lane & 7) * 128, fq * 1024);
        const int slot = ks < 12 ? ks : ks - 12;
#pragma unroll
        for (int v = 0; v < 4; ++v) {
#pragma unroll
          for (int n = 0; n < 3; ++n) {
            dma16(r1, L_W + v * L_WREG + slot * 3072 + n * 1024, vo[n], (64 * v + ks) * 2752);
            asm volatile("s_waitcnt vmcnt(%0)" ::"i"(WINL) : "memory");
          }
        }
        setw(Y, F_PROG, ks);
      }
    }
    else {
      __builtin_amdgcn_s_sleep(64);
#pragma unroll 1
      for (int n = 0; n < 6; ++n) touch(r2, min(43 * i32 + 8 * n + (lane >> 3), 43 * i32 + 42) * 4096 + (lane & 7) * 128, fq * 1024);
    }
    DRAIN();
    setw(Y, F_PROG, 16);
    STAMP(1);
    unsigned vo2[4];
#pragma unroll
    for (int n = 0; n < 4; ++n) {
      const int rho = 8 * n + (lane >> 3), pp = lane & 7;
      const int fr = 8 * ((rho >> 2) & 3) + 4 * (rho >> 4) + (rho & 3);
      vo2[n] = fr * 4096 + st * 128 + ((pp ^ (((rho >> 1) & 3) << 1)) * 16);
    }
    waitall_slow(Y, 9);
#pragma unroll 1
    for (int ks = 0; ks < 11; ++ks) {
      if (ks == 6) { setw(Y, F_PROG, 20); waitall_slow(Y, 11); }
      if (ks == 9) waitall_slow(Y, 12);
      const int so = ks < 6 ? 12288 + ks * 4096 : (ks < 9 ? (ks - 6) * 4096 : 12288 + (ks - 9) * 4096);
#pragma unroll
      for (int v = 0; v < 4; ++v) {
#pragma unroll
        for (int n = 0; n < 4; ++n) dma16(r2, L_W + v * L_WREG + so + n * 1024, vo2[n], min(11 * v + ks, 42) * (32 * 4096));
      }
      asm volatile("s_waitcnt vmcnt(32)" ::: "memory");
    }
    setw(Y, F_PROG, 25);
    DRAIN();
    setw(Y, F_PROG, 27);
    return;
  }

  const __amdgpu_buffer_rsrc_t rx = __builtin_amdgcn_make_buffer_rsrc((void*)x, 0, 2048 * 1024, 0x00020000);
  u4 xa = __builtin_amdgcn_raw_buffer_load_b128(rx, lane * 16, (s0 + 256 * u) * 1024, 0);
  FENCE();
  __builtin_amdgcn_s_barrier();
  FENCE();
  const unsigned* tabf = TABS.fwd[(c >> 2) == g ? (c & 3) : 4];
  const u4 AC0 = *(const u4*)tabf, AC1 = *(const u4*)(tabf + 4);
  const int pw = b >> 6, pt = (b >> 5) & 1, pp2 = pt ? (pw ? 8 - pw : 4) : pw, k2b = (b >> 1) & 15;
  const bool mir = k2b >= 8, sym = pw && !pt, spec = (b & 1) == 0 && (!sym || (k2b & 7) == 0);
  const bool emir = pw && (pt ? mir : !mir);
  u4 wraw; unsigned wex;
  {
    const __amdgpu_buffer_rsrc_t rw = __builtin_amdgcn_make_buffer_rsrc((void*)(u < 4 ? w_r : w_i), 0, ABL_WBYTES, 0x00020000);
    const int j = pp2 + 8 * (tid & 255), jm = (2048 - j) & 2047, kb = 8 * (b & 1) + 32 * k2b;
    wraw = __builtin_amdgcn_raw_buffer_load_b128(rw, (mir ? jm : j) * 514, (mir ? 505 - kb : kb) * 2, 0);
    const int ke = pw == 0 ? 16 * (k2b ? k2b : 16) : (pt ? (mir ? 496 - kb : kb + 16) : kb);
    wex = __builtin_amdgcn_raw_buffer_load_b16(rw, (emir ? jm : j) * 514, spec ? ke * 2 : OOB, 0);
  }
  __builtin_amdgcn_raw_buffer_store_b128(S4, rws, tid * 16, WS_HT + b * 8192, SC1);
  __builtin_amdgcn_raw_buffer_store_b128(S4, rws, tid * 16, b < 172 ? WS_G + b * 8192 : OOB, SC1);
  stx(S4, rws, tid * 16, WS_H + b * 8192);
  const int w = u >> 1, t = u & 1, pu = t ? (w ? 8 - w : 4) : w;
  const float tp = (float)(s0 * pu) * (1.f / 2048.f);
  const f4 kf = {(float)(4 * g), (float)(4 * g + 1), (float)(4 * g + 2), (float)(4 * g + 3)};
  const f4 nf = kf * (float)c;
  const f4 c16 = cos4(nf * (1.f / 16.f)), s16 = sin4(nf * (1.f / 16.f));
  const f4 c32 = cos4(nf * (1.f / 32.f)), s32 = sin4(nf * (1.f / 32.f));
  u4 FR = ziph(0.25f * c16, 0.25f * s16), FI = ziph(-0.25f * s16, 0.25f * c16);
  const float A1 = (w == 0 && t) ? 0.f : 1.f, A2 = w ? (t ? -1.f : 1.f) : 0.f, A3 = 1.f - A1, A4 = -A2;
  const f4 Z = {0.f, 0.f, 0.f, 0.f};

  {
    const f4 alt = {1.f, -1.f, 1.f, -1.f};
    const f4 cc = 0.125f * ((w && t && c == 0) ? alt : c32), ns = 0.125f * ((w == 0 && c == 0) ? alt : -s32);
    u4 Mr0 = ziph(A1 * cc, A2 * ns + A3 * cc), Mi0 = ziph(A1 * ns, A4 * cc + A3 * ns);
    const unsigned sg = (c & 1) ? 0x80008000u : 0u;
    u4 Mr1 = Mr0 ^ sg, Mi1 = Mi0 ^ sg;
    const f4 n16 = c ? nf : 16.f * kf;
    f4 ang = (t ? n16 : nf) * (1.f / 512.f) + tp;
    if (w == 0 && c == 0) ang = Z;
    f4 tc = cos4(ang), ts = sin4(ang);
    u4 GR = {}, GI = {};
    if (w == 0) {
      const f4 ps = n16 * (1.f / 32.f) + tp, gc = 0.25f * cos4(ps), gs = -0.25f * sin4(ps);
      GR = (c & 1) ? ziph(Z, gc) : ziph(gc, Z); GI = (c & 1) ? ziph(Z, gs) : ziph(gs, Z);
    }
    unsigned aXW = L_A + u * 1024 + ((16 * lane) ^ ((u & 3) << 6));
    unsigned aXR = L_A + q * 1024 + ((128 * u + 32 * g + 8 * p4) ^ (q << 6));
    unsigned aEW = L_A + (c >> 3) * 1024 + 128 * u + (c & 7) * 16 + 4 * g;
    unsigned aEW2 = aEW ^ 128;
    unsigned aER = L_A + (2 * w + (c >> 3)) * 1024 + ((128 * g) ^ ((w & 1) << 7)) + (c & 7) * 16;
    unsigned aST = (c * 4 + g) * 16;
    PIN(Mr0); PIN(Mi0); PIN(Mr1); PIN(Mi1); PIN(tc); PIN(ts); PIN(GR); PIN(GI); PIN(FR); PIN(FI);
    PIN(aXW); PIN(aXR); PIN(aEW); PIN(aEW2); PIN(aER); PIN(aST);
    STAMP(13);
    PIN(xa);
    const f4 va = {blo(xa[0]), bhi(xa[0]), blo(xa[1]), bhi(xa[1])}, vb = {blo(xa[2]), bhi(xa[2]), blo(xa[3]), bhi(xa[3])};
    { f4 tmp = va; PIN(tmp); } STAMP(1);
    const f4 s4v = va + vb, q4v = va * va + vb * vb;
    const f2 sq = swap32((s4v[0] + s4v[2]) + (s4v[1] + s4v[3]), (q4v[0] + q4v[2]) + (q4v[1] + q4v[3]));
    float tt = sq.x + sq.y;
    const f2 t16 = swap16(tt, tt);
    tt = row16_sum(t16.x + t16.y);
    const f2 tot = swap32(tt, tt);
    const float mean = tot.x * (1.f / 512.f);
    const float rs = __builtin_amdgcn_rsqf(fmaxf(fmaf(-mean, mean, tot.y * (1.f / 512.f)), 0.f) + 1e-5f), mr = -mean * rs;
    const f4 na = va * rs + mr, nb = vb * rs + mr;
    const u4 xn = {pkh(na[0], na[1]), pkh(na[2], na[3]), pkh(nb[0], nb[1]), pkh(nb[2], nb[3])};
    *lp<u4>(aXW) = xn;
    sig(Y, 1);
    u4 bf;
    WAIT_READ(1, bf = cat(trrd(aXR), trrd(aXR + 4096)); PIN(bf));
    const f4 d0 = mfma_h(AC0, bf, Z), d1 = mfma_h(AC1, bf, Z);
    *lp<unsigned>(aEW) = pkh(d0[0], d0[1]);
    *lp<unsigned>(aEW2 + 2048) = pkh(d0[2], d0[3]);
    *lp<unsigned>(aEW + 4096) = pkh(d1[0], d1[1]);
    *lp<unsigned>(aEW2 + 6144) = pkh(d1[2], d1[3]);
    sig(Y, 2);
    const unsigned hl = *lp<volatile unsigned>(Y.base + F_HELP);
    u4 e0, e1;
    WAIT_READ(2, e0 = *lp<u4>(aER); e1 = *lp<u4>(aER + 512); PIN(e0); PIN(e1));
    f4 zr = mfma_h(e1, Mr1, mfma_h(e0, Mr0, Z)), zi = mfma_h(e1, Mi1, mfma_h(e0, Mi0, Z));
    cmulc(zr, zi, tc, ts);
    const u4 zp = ziph(zr, zi);
    const f4 gr = mfma_h(zp, GR, Z), gi = mfma_h(zp, GI, Z);
    f4 yr = mfma_h(zp, FR, Z), yi = mfma_h(zp, FI, Z);
    const bool sel = w == 0 && g == 0;
    yr[0] = sel ? gr[0] : yr[0]; yi[0] = sel ? gi[0] : yi[0];
    u4 ov = ziph(yr, yi);
    PIN(ov);
    if (__builtin_expect(__builtin_amdgcn_readfirstlane(hl) == 0, 0)) waitw(Y, F_HELP, 1);
    stx(ov, rws, aST, WS_X1 + b * 8192 + u * 1024);
    sig(Y, 3);
    *lp<float>(L_MEAN + 4 * u) = mean;
  }
  STAMP(2);
  u4 IR = zipb(c16, -s16), II = zipb(s16, c16);
  const int dn = 32 * (g >> 1) + 8 * (c >> 2) + 4 * (g & 1) + (c & 3);
  unsigned short xr[8];
#pragma unroll
  for (int r = 0; r < 8; ++r) xr[r] = __builtin_amdgcn_raw_buffer_load_b16(rx, dn * 2, (s0 + 256 * r) * 1024 + 128 * u, 0);
  {
    f4 tc = cos4(nf * (1.f / 256.f)), ts = sin4(nf * (1.f / 256.f));
    f4 wre, wim;
    if (spec) {
      if (u >= 4 && emir != mir) wex ^= 0x8000u;
      if (sym) wex = pkb(0.5f * (blo(wex) + (mir ? bhi(wraw[3]) : blo(wraw[0]))), 0.f);
      if (mir) wraw[3] = (wraw[3] & 0xFFFFu) | (wex << 16); else wraw[0] = (wraw[0] & 0xFFFF0000u) | wex;
    }
    waitall(Y, 3);
    *lp<u4>(L_A + tid * 16) = wraw;
    sig(Y, 15); waitall(Y, 15);
    {
      const unsigned pr = L_A + c * 16 + g * 1024 + 2 * (mir ? 7 - u : u);
#pragma unroll
      for (int i = 0; i < 4; ++i) { wre[i] = blo(*lp<unsigned short>(pr + i * 256)); wim[i] = blo(*lp<unsigned short>(pr + i * 256 + 4096)); }
      if (mir) wim = -wim;
    }
    sig(Y, 16);
    unsigned aLD = (tid >> 1) * 8192 + (tid & 1) * 16;
    unsigned aPW = L_A + (tid & 1) * 4096 + (((tid >> 1) * 4) ^ ((tid & 1) << 6));
    unsigned aPR = L_A + u * 1024 + ((16 * lane) ^ ((u >> 2) << 6));
    unsigned aST = tid * 16;
    PIN(tc); PIN(ts); PIN(wre); PIN(wim); PIN(IR); PIN(II); PIN(aLD); PIN(aPW); PIN(aPR); PIN(aST);
    DRAIN();
    waitall(Y, 16);
    waitw(Y, F_HELP, 2);
    __builtin_amdgcn_s_sleep(PRE1);
    STAMP(14);
    u4 v;
#pragma unroll 1
    for (unsigned s = 0; s < SPIN_CAP; ++s) { FENCE(); v = ldx(rws, aLD, WS_X1 + b * 32); if (!anysent(mx(0u, v))) break; }
    STAMP(3);
#pragma unroll
    for (int i = 0; i < 4; ++i) *lp<unsigned>(aPW + i * 1024) = v[i];
    sig(Y, 4);
    u4 a;
    WAIT_READ(4, a = *lp<u4>(aPR); PIN(a));
    f4 r = mfma_h(a, FR, Z), im = mfma_h(a, FI, Z);
    cmulc(r, im, tc, ts);
    u4 z = ziph(r, im);
    r = mfma_h(FR, z, Z); im = mfma_h(FI, z, Z);
    cmul(r, im, wre, wim);
    z = zipb(r, im);
    r = mfma_b(z, IR, Z); im = mfma_b(z, II, Z);
    cmul(r, im, tc, ts);
    z = zipb(r, im);
    r = mfma_b(IR, z, Z); im = mfma_b(II, z, Z);
    *lp<u4>(aPR) = zipb(r, im);
    sig(Y, 5);
    u4 ov;
    WAIT_READ(5, ov[0] = *lp<unsigned>(aPW); ov[1] = *lp<unsigned>(aPW + 1024); ov[2] = *lp<unsigned>(aPW + 2048); ov[3] = *lp<unsigned>(aPW + 3072); PIN(ov));
    stx(ov, rws, aST, WS_X2 + b * 8192);
  }
  STAMP(4);
  u4 WA[4][3];
  const unsigned a1off = L_W + w * L_WREG + (4 * g + q) * 96;
  if (g1) {
    waitw(Y, F_PROG, 4);
#pragma unroll
    for (int kk = 0; kk < 4; ++kk)
#pragma unroll
      for (int nt = 0; nt < 3; ++nt) {
        const unsigned off = a1off + kk * 3072 + (nt < 2 ? 16 * p4 + 8 * nt : 64 + 8 * p4);
        WA[kk][nt] = cat(trrd(off), trrd(off + 1536));
      }
  }
  sig(Y, 6);
  {
    const unsigned* tabi = TABS.inv[(c >> 2) == g ? (c & 3) : 4];
    u4 AI00 = *(const u4*)tabi, AI01 = *(const u4*)(tabi + 4), AI10 = *(const u4*)(tabi + 8), AI11 = *(const u4*)(tabi + 12);
    const f4 m0 = *lp<f4>(L_MEAN), m1 = *lp<f4>(L_MEAN + 16);
    f4 xc0 = {blo(xr[0]), blo(xr[1]), blo(xr[2]), blo(xr[3])}, xc1 = {blo(xr[4]), blo(xr[5]), blo(xr[6]), blo(xr[7])};
    xc0 -= m0; xc1 -= m1;
    f4 n16 = nf; if (g == 0) n16[0] = 16.f * (float)c;
    f4 ang = (t ? n16 : nf) * (1.f / 512.f) + tp;
    if (w == 0 && g == 0) ang[0] = 0.f;
    f4 tc = cos4(ang), ts = sin4(ang);
    u4 GR = {}, GI = {};
    if (w == 0) {
      const f4 ps = n16 * (1.f / 32.f) + tp; f4 gc = cos4(ps), gs = -sin4(ps);
      if (g == 0) { gc[0] *= 0.5f; gs[0] *= 0.5f; }
      const u4 gg = zipb(gc, gs);
      GR[0] = gg[0]; GR[2] = gg[2]; GI[1] = gg[1]; GI[3] = gg[3];
    }
    f4 cc = c32, ns = -s32;
    { const float alt = (c & 1) ? -1.f : 1.f; if (g == 0) { if (w && t) cc[0] = alt; if (w == 0) ns[0] = alt; } }
    u4 Me0 = zipb(A1 * cc, A1 * ns), Mo0 = zipb(A2 * ns + A3 * cc, A4 * cc + A3 * ns);
    const u4 SG = {0u, 0x80008000u, 0u, 0x80008000u};
    u4 Me1 = Me0 ^ SG, Mo1 = Mo0 ^ SG;
    unsigned aLD = (8 * g + (c >> 3)) * 8192 + (c & 7) * 4;
    const int re_ = 2 * u, ro_ = u ? 2 * u + 1 : 2;
    const unsigned wo = 32 * c + 8 * (g ^ (c >> 2));
    unsigned aEWe = L_E + re_ * 1024 + (wo ^ ((re_ & 3) << 6)), aEWo = L_E + ro_ * 1024 + (wo ^ ((ro_ & 3) << 6));
    const unsigned ro = 128 * u + 64 * (g >> 1) + 32 * (p4 >> 1) + 8 * ((2 * (p4 & 1) + (g & 1)) ^ (u & 3));
    unsigned aER = L_E + q * 1024 + (ro ^ (q << 6)), aER0 = q == 1 ? aER + 1024 : aER;
    unsigned aPW = L_PART + u * 32 + (lane >> 3) * 4, aPR = L_PART + lane * 4, aST = lane * 16;
    PIN(AI00); PIN(AI01); PIN(AI10); PIN(AI11); PIN(xc0); PIN(xc1); PIN(tc); PIN(ts); PIN(GR); PIN(GI); PIN(Me0); PIN(Mo0); PIN(Me1); PIN(Mo1);
    PIN(aLD); PIN(aEWe); PIN(aEWo); PIN(aER); PIN(aER0); PIN(aPW); PIN(aPR); PIN(aST);
    DRAIN();
    waitall(Y, 6);
    __builtin_amdgcn_s_sleep(PRE2);
    STAMP(15);
    u4 z;
#pragma unroll 1
    for (unsigned s = 0; s < SPIN_CAP; ++s) {
      FENCE();
#pragma unroll
      for (int i = 0; i < 4; ++i) z[i] = ldw(rws, aLD, WS_X2 + b * 32 + (u * 32 + 2 * i) * 8192);
      if (!anysent(mx(0u, z))) break;
    }
    STAMP(5);
    const f4 gr = mfma_b(z, GR, Z), gi = mfma_b(z, GI, Z);
    f4 r = mfma_b(z, IR, Z), im = mfma_b(z, II, Z);
    const bool sel = w == 0 && g == 0;
    r[0] = sel ? gr[0] : r[0]; im[0] = sel ? gi[0] : im[0];
    cmul(r, im, tc, ts);
    const u4 U = zipb(r, im);
    const f4 e0 = mfma_b(U, Me0, Z), o0 = mfma_b(U, Mo0, Z), e1 = mfma_b(U, Me1, Z), o1 = mfma_b(U, Mo1, Z);
    const u4 pe = catb(e0, e1), po = catb(o0, o1);
    *lp<u2>(aEWe) = pe.xy; *lp<u2>(aEWo) = po.xy;
    *lp<u2>(aEWe + 512) = pe.zw; *lp<u2>(aEWo + 512) = po.zw;
    sig(Y, 7);
    u4 k0, k1;
    WAIT_READ(7, k0 = cat(trrd(aER0), trrd(aER + 4096)); k1 = cat(trrd(aER + 8192), trrd(aER + 12288)); PIN(k0); PIN(k1));
    const f4 h0 = mfma_b(AI01, k1, mfma_b(AI00, k0, xc0)), h1 = mfma_b(AI11, k1, mfma_b(AI10, k0, xc1));
    const f4 q0 = h0 * h0, q1 = h1 * h1;
    const f2 a0 = swap32(q0[0], q1[0]), a1 = swap32(q0[1], q1[1]), a2 = swap32(q0[2], q1[2]), a3 = swap32(q0[3], q1[3]);
    const f2 b0 = swap16(a0.x + a0.y, a2.x + a2.y), b1 = swap16(a1.x + a1.y, a3.x + a3.y);
    float pr;
    asm volatile("s_nop 1\n\tv_add_f32_dpp %0, %1, %1 row_ror:8 row_mask:0xf bank_mask:0x3\n\tv_add_f32_dpp %0, %2, %2 row_ror:8 row_mask:0xf bank_mask:0xc\n\t"
                 "s_nop 1\n\tv_add_f32_dpp %0, %0, %0 row_half_mirror row_mask:0xf bank_mask:0xf\n\t"
                 "s_nop 1\n\tv_add_f32_dpp %0, %0, %0 quad_perm:[2,3,0,1] row_mask:0xf bank_mask:0xf\n\t"
                 "s_nop 1\n\tv_add_f32_dpp %0, %0, %0 quad_perm:[1,0,3,2] row_mask:0xf bank_mask:0xf"
                 : "=&v"(pr) : "v"(b0.x + b0.y), "v"(b1.x + b1.y));
    *lp<float>(aPW) = pr;
    sig(Y, 8);
    float ps;
    WAIT_READ(8, ps = *lp<float>(aPR); PIN(ps));
    ps = ror_add<0x128>(ps);
    const f2 p16 = swap16(ps, ps); ps = p16.x + p16.y;
    const f2 p32 = swap32(ps, ps); ps = p32.x + p32.y;
    const int rsv = __builtin_bit_cast(int, __builtin_amdgcn_rsqf(fmaf(ps, (1.f + 1e-5f) / 512.f, 1e-10f)));
    f4 r0, r1;
#pragma unroll
    for (int i = 0; i < 4; ++i) { r0[i] = __builtin_bit_cast(float, __builtin_amdgcn_readlane(rsv, i)); r1[i] = __builtin_bit_cast(float, __builtin_amdgcn_readlane(rsv, 4 + i)); }
    stx(catb(h0 * r0, h1 * r1), rws, aST, WS_HT + u * 262144 + b * 1024);
    STAMP(6);
    FENCE();
    *lp<u4>(L_A + u * 1024 + dn * 16) = catb(h0 + m0, h1 + m1);
    FENCE();
    const unsigned ph = L_A + u * 1024 + (16 * g + 8 * (p4 >> 1) + q) * 16 + 8 * (p4 & 1);
    stx(cat(trrd(ph), trrd(ph + 64)), rws, (c & 7) * 262144 + (2 * g + (c >> 3)) * 16, WS_H + s0 * 1024 + u * 128);
  }
  const unsigned vo = g * 1024 + c * 16 + t * 512;
  if (g1) {
    u4 braw;
    if (u < 4) braw = *(const u4*)(b1 + f0 + 8 * g); else braw.xy = *(const u2*)(b1 + f0 + 32 + 4 * g);
    DRAIN();
    __builtin_amdgcn_s_sleep(PRE3);
    const unsigned hb = WS_HT + dt * 262144 + w * 65536, cano = lane * 1024 + t * 512 + 508;
    u4 B[4][2];
#pragma unroll 1
    for (unsigned s = 0; s < SPIN_CAP; ++s) {
      FENCE();
      const unsigned can = ldw(rws, cano, hb);
#pragma unroll
      for (int kk = 0; kk < 4; ++kk)
#pragma unroll
        for (int mt = 0; mt < 2; ++mt) { B[kk][mt] = ldx(rws, vo + mt * 256, hb + kk * 4096); __builtin_amdgcn_sched_barrier(0); }
      if (!anysent(can)) break;
#pragma unroll 1
      for (; s < SPIN_CAP; ++s) { FENCE(); if (!anysent(ldw(rws, cano, hb))) break; }
    }
    STAMP(7);
    f4 acc[3][2];
#pragma unroll
    for (int nt = 0; nt < 3; ++nt) { acc[nt][0] = Z; acc[nt][1] = Z; }
#pragma unroll
    for (int n = 0; n < 4; ++n) {
      if (n < 3) waitw(Y, F_PROG, 4 * n + 8);
      const unsigned slot = a1off + (n < 2 ? (4 * n + 4) * 3072 : 0);
#pragma unroll
      for (int kk = 0; kk < 4; ++kk) {
        if (__builtin_expect(anysent(mx(mx(0u, B[kk][0]), B[kk][1])), 0)) reload2(B[kk], rws, vo, hb + (4 * n + kk) * 4096);
#pragma unroll
        for (int mt = 0; mt < 2; ++mt)
#pragma unroll
          for (int nt = 0; nt < 3; ++nt) acc[nt][mt] = mfma_b(WA[kk][nt], B[kk][mt], acc[nt][mt]);
        if (n < 3) {
#pragma unroll
          for (int mt = 0; mt < 2; ++mt) B[kk][mt] = ldx(rws, vo + mt * 256, hb + (4 * n + kk + 4) * 4096);
#pragma unroll
          for (int nt = 0; nt < 3; ++nt) {
            const unsigned off = slot + kk * 3072 + (nt < 2 ? 16 * p4 + 8 * nt : 64 + 8 * p4);
            WA[kk][nt] = cat(trrd(off), trrd(off + 1536));
          }
        }
        if (n == 3 && kk == 2) waitall(Y, 9);
        __builtin_amdgcn_sched_barrier(0);
      }
      if (n == 2) sig(Y, 9);
    }
    STAMP(8);
#pragma unroll
    for (int mt = 0; mt < 2; ++mt)
#pragma unroll
      for (int nt = 0; nt < 3; ++nt) *lp<f4>(L_W + w * L_WREG + ((2 * t + mt) * 3 + nt) * 1024 + lane * 16) = acc[nt][mt];
    sig(Y, 10);
    const unsigned gbase = WS_G + (dt * 172 + (f0 >> 3)) * 1024 + (u & 3) * 256;
    const unsigned pr = L_W + ((u & 3) * 3 + (u >> 2) * 2) * 1024 + lane * 16;
    auto gelu = [](f4 v) {
      const f4 z = v * (v * v * -0.10294324f - 2.3022082f);
      f4 e = {__builtin_amdgcn_exp2f(z[0]), __builtin_amdgcn_exp2f(z[1]), __builtin_amdgcn_exp2f(z[2]), __builtin_amdgcn_exp2f(z[3])};
      e += 1.f;
      const f4 r = {__builtin_amdgcn_rcpf(e[0]), __builtin_amdgcn_rcpf(e[1]), __builtin_amdgcn_rcpf(e[2]), __builtin_amdgcn_rcpf(e[3])};
      return v * r;
    };
    const f4 bA = {blo(braw[0]), bhi(braw[0]), blo(braw[1]), bhi(braw[1])};
    if (u < 4) {
      const f4 bB = {blo(braw[2]), bhi(braw[2]), blo(braw[3]), bhi(braw[3])};
      f4 pa[4], pb[4];
      waitall(Y, 10);
#pragma unroll
      for (int v = 0; v < 4; ++v) { pa[v] = *lp<f4>(pr + v * L_WREG); pb[v] = *lp<f4>(pr + v * L_WREG + 1024); }
      sig(Y, 11);
      stx(catb(gelu(((pa[0] + bA) + pa[1]) + (pa[2] + pa[3])), gelu(((pb[0] + bB) + pb[1]) + (pb[2] + pb[3]))), rws, g * 1024 + c * 16, gbase);
    } else {
      f4 pa[4];
      waitall(Y, 10);
#pragma unroll
      for (int v = 0; v < 4; ++v) pa[v] = *lp<f4>(pr + v * L_WREG);
      sig(Y, 11);
      const f4 gv = gelu(((pa[0] + bA) + pa[1]) + (pa[2] + pa[3]));
      const u2 gb = {pkb(gv[0], gv[1]), pkb(gv[2], gv[3])};
      __builtin_amdgcn_raw_buffer_store_b64(gb, rws, (4 + (g >> 1)) * 1024 + c * 16 + (g & 1) * 8, gbase, SC1);
    }
  } else {
    sig(Y, 9); sig(Y, 11);
    __builtin_amdgcn_s_sleep(100);
  }
  STAMP(9);
  {
    const int srow = st * 64 + w * 16 + c;
    const float bias = blo(b2[srow]);
    const unsigned hvo = c * 1024 + g * 16, hso = WS_H + (st * 64 + w * 16) * 1024 + dt * 128 + t * 64;
    u4 hv = ldx(rws, hvo, hso);
    u4 WB[2][4];
    const int rho = 4 * g + q;
    const unsigned b2off = L_W + w * L_WREG + rho * 128 + 8 * p4, sw = ((rho >> 1) & 3) ^ w;
    waitw(Y, F_PROG, 20);
#pragma unroll
    for (int kk = 0; kk < 2; ++kk)
#pragma unroll
      for (int nt = 0; nt < 4; ++nt) { const unsigned off = b2off + 12288 + kk * 4096 + ((nt ^ sw) * 32); WB[kk][nt] = cat(trrd(off), trrd(off + 2048)); }
    sig(Y, 12);
    DRAIN();
#pragma unroll 1
    for (unsigned s = 0; s < SPIN_CAP && anysent(mx(0u, hv)); ++s) { FENCE(); hv = ldx(rws, hvo, hso); }
    f4 ha = {blo(hv[0]) + bias, bhi(hv[0]) + bias, blo(hv[1]) + bias, bhi(hv[1]) + bias}, hb4 = {blo(hv[2]) + bias, bhi(hv[2]) + bias, blo(hv[3]) + bias, bhi(hv[3]) + bias};
    PIN(ha); PIN(hb4);
    __builtin_amdgcn_s_sleep(PRE4);
    const unsigned gb = WS_G + (dt * 172 + w * 44) * 1024, glast = gb + (w == 3 ? 9 : 10) * 4096;
    const unsigned cano = min(lane, w == 3 ? 39 : 43) * 1024 + t * 512 + 508;
    u4 A[4][2];
#pragma unroll 1
    for (unsigned s = 0; s < SPIN_CAP; ++s) {
      FENCE();
      const unsigned can = ldw(rws, cano, gb);
#pragma unroll
      for (int kk = 0; kk < 4; ++kk)
#pragma unroll
        for (int mt = 0; mt < 2; ++mt) { A[kk][mt] = ldx(rws, vo + mt * 256, gb + kk * 4096); __builtin_amdgcn_sched_barrier(0); }
      if (!anysent(can)) break;
#pragma unroll 1
      for (; s < SPIN_CAP; ++s) { FENCE(); if (!anysent(ldw(rws, cano, gb))) break; }
    }
    STAMP(10);
    f4 acc[2][4];
#pragma unroll
    for (int nt = 0; nt < 4; ++nt) { acc[0][nt] = Z; acc[1][nt] = Z; }
#pragma unroll
    for (int ks = 0; ks < 11; ++ks) {
      const int kk = ks & 3;
      if (ks == 2) waitw(Y, F_PROG, 25);
      if (ks == 7) waitw(Y, F_PROG, 27);
      if (ks == 10) waitall(Y, 13);
      if (__builtin_expect(anysent(mx(mx(0u, A[kk][0]), A[kk][1])), 0)) reload2(A[kk], rws, vo, ks == 10 ? glast : gb + ks * 4096);
      if (ks == 10) { const unsigned km = w == 3 ? 0u : ~0u; A[kk][0] &= km; A[kk][1] &= km; }
#pragma unroll
      for (int mt = 0; mt < 2; ++mt)
#pragma unroll
        for (int nt = 0; nt < 4; ++nt) acc[mt][nt] = mfma_b(A[kk][mt], WB[ks & 1][nt], acc[mt][nt]);
      if (ks + 4 < 11) {
#pragma unroll
        for (int mt = 0; mt < 2; ++mt) A[kk][mt] = ldx(rws, vo + mt * 256, ks + 4 == 10 ? glast : gb + (ks + 4) * 4096);
      }
      if (ks + 2 < 11) {
        const int k4 = ks + 2, so = k4 < 6 ? 12288 + k4 * 4096 : (k4 < 9 ? (k4 - 6) * 4096 : 12288 + (k4 - 9) * 4096);
#pragma unroll
        for (int nt = 0; nt < 4; ++nt) { const unsigned off = b2off + so + ((nt ^ sw) * 32); WB[ks & 1][nt] = cat(trrd(off), trrd(off + 2048)); }
      }
      if (ks == 8) sig(Y, 13);
      __builtin_amdgcn_sched_barrier(0);
    }
    STAMP(11);
#pragma unroll
    for (int nt = 1; nt < 4; ++nt)
#pragma unroll
      for (int mt = 0; mt < 2; ++mt) *lp<f4>(L_W + w * L_WREG + (nt * 4 + 2 * t + mt) * 1024 + lane * 16) = acc[mt][nt];
    sig(Y, 14);
    ha += acc[0][0]; hb4 += acc[1][0];
    f4 pa[4], pb[4];
    waitall(Y, 14);
#pragma unroll
    for (int v = 1; v < 4; ++v) { const unsigned pr = L_W + (w ^ v) * L_WREG + (v * 4 + 2 * t) * 1024 + lane * 16; pa[v] = *lp<f4>(pr); pb[v] = *lp<f4>(pr + 1024); }
    *(u4*)(out + (size_t)srow * 512 + dt * 64 + t * 32 + g * 8) = catb((ha + pa[1]) + (pa[2] + pa[3]), (hb4 + pb[1]) + (pb[2] + pb[3]));
  }
  STAMP(12);
  ts1 = 0;
  if (u == 0 && ts1 && (TBLK < 0 || (int)blockIdx.x == TBLK)) { while (rtc() - ts0 < 6000 + 10 * (ts1 - ts0)) __builtin_amdgcn_s_sleep(2); }
}

__global__ void k_first(unsigned long long* p) { unsigned long long t = rtc(); if (threadIdx.x == 0 && blockIdx.x == 0) *p = t; }
__global__ void k_last(unsigned long long* p) { unsigned long long t0 = *(volatile unsigned long long*)p; while (rtc() - t0 < 6000) __builtin_amdgcn_s_sleep(2); }
extern "C" void kernel_launch(void* const* d_in, const int* in_sizes, int n_in, void* d_out, int out_size, void* d_ws, size_t ws_size,
                              hipStream_t stream) {
  (void)hipFuncSetAttribute((const void*)fused_kernel, hipFuncAttributeMaxDynamicSharedMemorySize, L_BYTES);
  k_first<<<XKB, 64, 0, stream>>>((unsigned long long*)((char*)d_ws + (32 << 20)));
  fused_kernel<<<256, 640, L_BYTES, stream>>>((const unsigned short*)d_in[0], (const unsigned short*)d_in[1], (const unsigned short*)d_in[2],
                                              (const unsigned short*)d_in[4], (const unsigned short*)d_in[5], (const unsigned short*)d_in[6],
                                              (const unsigned short*)d_in[7], (unsigned short*)d_out, (unsigned char*)d_ws);
}
```

```cpp
#include <hip/hip_runtime.h>
#include <hip/hip_bf16.h>

typedef _Float16 h8 __attribute__((ext_vector_type(8)));
typedef _Float16 h2 __attribute__((ext_vector_type(2)));
typedef __bf16 b8 __attribute__((ext_vector_type(8)));
typedef __bf16 b2 __attribute__((ext_vector_type(2)));
typedef float f2 __attribute__((ext_vector_type(2)));
typedef float f4 __attribute__((ext_vector_type(4)));
typedef float f16v __attribute__((ext_vector_type(16)));
typedef short s4 __attribute__((ext_vector_type(4)));
typedef unsigned u2 __attribute__((ext_vector_type(2)));
typedef unsigned u4 __attribute__((ext_vector_type(4)));
typedef unsigned long long u64;
typedef __amdgpu_buffer_rsrc_t rsrc_t;
#define LAS __attribute__((address_space(3)))
#define DEV __device__ __forceinline__
#define FENCE() asm volatile("" ::: "memory")
#define PIN(x) asm volatile("" : "+v"(x))
#define PIN2(a, b) asm volatile("" : "+v"(a), "+v"(b))
#define PIN4(a, b, c, d) asm volatile("" : "+v"(a), "+v"(b), "+v"(c), "+v"(d))
#define PINS(x) asm volatile("" : "+s"(x))
#define VMWAIT(n) asm volatile("s_waitcnt vmcnt(%0)" ::"i"(n) : "memory")

#define SW 0
#define ABL_W1 (2048 * 1376 * 2)
#define ABL_W2 (2048 * 1376 * 2)
#define ABL_W (2048 * 257 * 2)
#define ABL_CLR 0
#define CLRA 0
#define CLRB 4
#define PREH 850
#define SCHED "m+mDmmmmmm4nP523ExG8_dc9R67Tafbexx)V__yy@yIopUqrsLMwuvt$*"
#define XD1 1
#define E1 2
#define ABL_HT 0
#define ABL_G 0
#define W1 12
#define W2 8
#define MX mx2
#define WBE 1
#define MW 1
#define R1M 0
#define R2M 0
#define FILL 3
#define LB 0
#define WLATE 0
#define STAMPOP "s_memtime"
#define OUTAUX 16
#define CBX 28
#define IDLE 10500
#define HFE 0
#define SHADOW 1
#define CSL 1024
#define LSPIN 1
#define HLAY 1
#define FULL 1
#define SEEDEND 0
#define TM 8
#define PD4 1
#define TM2 0
#define V1 6
#define V2 6
#define ZDUP 0
#define STAMPS 0
#define PRE1 1600
#define PRE2 1350
#define PRE3 2000
#define PRE4 1300
#define PRE5 2400
#define WINA 4
#define WINB 6
#define WINC 6
#define WIND 8
#define WAUX 0
#define GATE0 1
#define GATE1B 0
#define GATE1 0
#define GATE2 0
#define CODE_BYTES 24576

#define NTV 0
constexpr int SC1 = 16, NT = NTV;
constexpr unsigned SENT = 0xFFFFFFFFu, OOB = 0x7FFF0000u;
constexpr unsigned TAGM = 0x00070007u, TAGMID = 0x00040004u;
constexpr unsigned SPIN_CAP = 1u << 13;
constexpr unsigned WS_X1 = 0, WS_X2 = 2u << 20, WS_HT = 4u << 20, WS_H = 6u << 20, WS_G = 8u << 20, WS_P = 10u << 20;
constexpr unsigned WS_SEED = 16u << 20, WS_HF = WS_SEED + 4096, WS_END = WS_HF + 32768;
constexpr unsigned WS_STAMP = 20u << 20;
constexpr unsigned L_W1 = 0, L_W2 = 98304, L_WF = 13 * 6144, L_TAB = L_WF + 16384, L_B1 = L_TAB + 1024, L_B2 = L_B1 + 256, L_E = 147456, L_A = L_E + 8192, L_OWN = L_E + 4096, L_BYTES = L_E + 16384;
constexpr unsigned L_FLAG = L_E + 1024, F_PROG = 192, L_ZERO = L_FLAG + 176  , L_MEAN = L_FLAG + 256, L_CO = L_FLAG + 320, L_PART = L_FLAG + 512, L_SINK = L_FLAG + 768;

struct Tabs { unsigned inv[5][16]; };
constexpr unsigned short cs8(int n, unsigned short one, unsigned short rt) {
  n &= 7; if (n == 2 || n == 6) return 0; const unsigned short m = (n & 1) ? rt : one; return (n >= 3 && n <= 5) ? (unsigned short)(m | 0x8000) : m;
}
constexpr unsigned short comb(int w, int eo, int r, unsigned short one, unsigned short rt) { return w == 0 ? cs8(4 * eo * r, one, rt) : cs8(r * w + 2 * eo, one, rt); }
constexpr Tabs make_tabs() {
  Tabs t = {};
  for (int i = 0; i < 4; ++i) {
    for (int h = 0; h < 2; ++h) for (int j = 0; j < 8; ++j) { const int r = 4 * h + i, w = j >> 1, tt = j & 1;
      unsigned e = comb(w, 0, r, 0x3800, 0x37B5), o = comb(w, 1, r, 0x3800, 0x37B5);
      if (w == 0) { if (tt) e = 0; else o = 0; }
      t.inv[i][8 * h + j] = e | (o << 16); }
  }
  return t;
}
__device__ const Tabs __attribute__((aligned(16))) TABS = make_tabs();

DEV f4 mfma_h(u4 a, u4 b, f4 c) { return __builtin_amdgcn_mfma_f32_16x16x32_f16(__builtin_bit_cast(h8, a), __builtin_bit_cast(h8, b), c, 0, 0, 0); }
DEV f4 mfma_b(u4 a, u4 b, f4 c) { return __builtin_amdgcn_mfma_f32_16x16x32_bf16(__builtin_bit_cast(b8, a), __builtin_bit_cast(b8, b), c, 0, 0, 0); }
DEV f16v mfma32(u4 a, u4 b, f16v c) { return __builtin_amdgcn_mfma_f32_32x32x16_bf16(__builtin_bit_cast(b8, a), __builtin_bit_cast(b8, b), c, 0, 0, 0); }
DEV unsigned pkh(float a, float b) { f2 v = {a, b}; return __builtin_bit_cast(unsigned, __builtin_convertvector(v, h2)); }
DEV unsigned pkb(float a, float b) { f2 v = {a, b}; return __builtin_bit_cast(unsigned, __builtin_convertvector(v, b2)); }
DEV u4 ziph(f4 a, f4 b) { u4 r = {pkh(a[0], b[0]), pkh(a[1], b[1]), pkh(a[2], b[2]), pkh(a[3], b[3])}; return r; }
DEV u4 ziphn(f4 a, f4 b) { u4 r = {pkh(-a[0], b[0]), pkh(-a[1], b[1]), pkh(-a[2], b[2]), pkh(-a[3], b[3])}; return r; }
DEV u4 zipbn(f4 a, f4 b) { u4 r = {pkb(a[0], -b[0]), pkb(a[1], -b[1]), pkb(a[2], -b[2]), pkb(a[3], -b[3])}; return r; }
DEV u4 zipb(f4 a, f4 b) { u4 r = {pkb(a[0], b[0]), pkb(a[1], b[1]), pkb(a[2], b[2]), pkb(a[3], b[3])}; return r; }
DEV u4 cath(f4 a, f4 b) { u4 r = {pkh(a[0], a[1]), pkh(a[2], a[3]), pkh(b[0], b[1]), pkh(b[2], b[3])}; return r; }
DEV u4 catb(f4 a, f4 b) { u4 r = {pkb(a[0], a[1]), pkb(a[2], a[3]), pkb(b[0], b[1]), pkb(b[2], b[3])}; return r; }
DEV float blo(unsigned u) { return __builtin_bit_cast(float, u << 16); }
DEV float bhi(unsigned u) { return __builtin_bit_cast(float, u & 0xFFFF0000u); }
DEV f4 unb(unsigned a, unsigned b) { f4 r = {blo(a), bhi(a), blo(b), bhi(b)}; return r; }
DEV f4 addh(f4 s, unsigned a, unsigned b) { const h2 x = __builtin_bit_cast(h2, a), y = __builtin_bit_cast(h2, b); f4 r = {s[0] + (float)x[0], s[1] + (float)x[1], s[2] + (float)y[0], s[3] + (float)y[1]}; return r; }
DEV unsigned add3h(unsigned a, unsigned b, unsigned c) { return __builtin_bit_cast(unsigned, (__builtin_bit_cast(h2, a) + __builtin_bit_cast(h2, b)) + __builtin_bit_cast(h2, c)); }
DEV f4 gelu(f4 v) {
  const f4 z = v * (v * v * -0.10294324f - 2.3022082f);
  f4 e = {__builtin_amdgcn_exp2f(z[0]), __builtin_amdgcn_exp2f(z[1]), __builtin_amdgcn_exp2f(z[2]), __builtin_amdgcn_exp2f(z[3])};
  e += 1.f;
  const f4 r = {__builtin_amdgcn_rcpf(e[0]), __builtin_amdgcn_rcpf(e[1]), __builtin_amdgcn_rcpf(e[2]), __builtin_amdgcn_rcpf(e[3])};
  return v * r;
}
DEV float dot2(unsigned a, unsigned b, float c) { return __builtin_amdgcn_fdot2_f32_bf16(__builtin_bit_cast(b2, a), __builtin_bit_cast(b2, b), c, false); }
DEV f4 cos4(f4 r) { f4 o = {__builtin_amdgcn_cosf(r[0]), __builtin_amdgcn_cosf(r[1]), __builtin_amdgcn_cosf(r[2]), __builtin_amdgcn_cosf(r[3])}; return o; }
DEV f4 sin4(f4 r) { f4 o = {__builtin_amdgcn_sinf(r[0]), __builtin_amdgcn_sinf(r[1]), __builtin_amdgcn_sinf(r[2]), __builtin_amdgcn_sinf(r[3])}; return o; }
template <class T> DEV LAS T* lp(unsigned off) { return (LAS T*)(size_t)off; }
DEV u2 trrd(unsigned off) { return __builtin_bit_cast(u2, __builtin_amdgcn_ds_read_tr16_b64_v4i16(lp<s4>(off))); }
DEV u4 cat(u2 a, u2 b) { u4 r = {a.x, a.y, b.x, b.y}; return r; }
DEV unsigned mx(unsigned m, u4 v) { return max(max(max(m, v.x), v.y), max(v.z, v.w)); }
DEV unsigned mx2(unsigned m, u4 v) { return max(max(m, v.y), v.w); }
DEV bool anysent(unsigned m) { return __builtin_amdgcn_ballot_w64(m == SENT) != 0; }
DEV unsigned tagdiff(unsigned m, u4 v, unsigned T) { return (((m | (v.x ^ T)) | (v.y ^ T)) | (v.z ^ T)) | (v.w ^ T); }
DEV bool anyuntagged(unsigned m) { return __builtin_amdgcn_ballot_w64((m & TAGM) != 0) != 0; }
DEV rsrc_t mkrsrc(const void* p, unsigned bytes) { return __builtin_amdgcn_make_buffer_rsrc((void*)p, 0, bytes, 0x00020000); }
DEV u4 ldx(rsrc_t r, unsigned v, unsigned s) { return __builtin_amdgcn_raw_buffer_load_b128(r, v, s, SC1); }
DEV unsigned ldw(rsrc_t r, unsigned v, unsigned s) { return __builtin_amdgcn_raw_buffer_load_b32(r, v, s, SC1); }
DEV void stx(u4 d, rsrc_t r, unsigned v, unsigned s) {
  __builtin_amdgcn_raw_buffer_store_b128(d, r, v, s, SC1);
  asm volatile("s_nop 1" ::"v"(d) : "memory");
}
template <int C> DEV float ror_add(float v) { return v + __builtin_bit_cast(float, __builtin_amdgcn_update_dpp(0, __builtin_bit_cast(int, v), C, 0xF, 0xF, true)); }
template <int N> DEV float bcast(int v) { return __builtin_bit_cast(float, __builtin_amdgcn_update_dpp(0, v, 0x150 + N, 0xF, 0xF, true)); }
DEV float row16_sum(float t) { t = ror_add<0x128>(t); t = ror_add<0x124>(t); t = ror_add<0x122>(t); return ror_add<0x121>(t); }
DEV f2 swap32(float a, float b) { auto r = __builtin_amdgcn_permlane32_swap(__builtin_bit_cast(unsigned, a), __builtin_bit_cast(unsigned, b), false, false); const unsigned r0 = r[0], r1 = r[1]; f2 o = {__builtin_bit_cast(float, r0), __builtin_bit_cast(float, r1)}; return o; }
DEV f2 swap16(float a, float b) { auto r = __builtin_amdgcn_permlane16_swap(__builtin_bit_cast(unsigned, a), __builtin_bit_cast(unsigned, b), false, false); const unsigned r0 = r[0], r1 = r[1]; f2 o = {__builtin_bit_cast(float, r0), __builtin_bit_cast(float, r1)}; return o; }
DEV void cmul(f4& r, f4& i, f4 c, f4 s) { const f4 t = r * c - i * s; i = r * s + i * c; r = t; }
DEV void cmulc(f4& r, f4& i, f4 c, f4 s) { const f4 t = r * c + i * s; i = i * c - r * s; r = t; }
DEV unsigned clk() { return (unsigned)__builtin_amdgcn_s_memtime(); }
DEV void pause(unsigned t0, int n) { for (int r = n - (int)(clk() - t0); r > 0; r -= 96) __builtin_amdgcn_s_sleep(1); }

template <int I> struct Ix { static constexpr int v = I; };
template <int I, int N, class F> DEV void static_for(F&& f) { if constexpr (I < N) { f(Ix<I>{}); static_for<I + 1, N>(f); } }
constexpr int g1_i(int n) { return n < 3 * (32 - TM) ? n / 3 : 32 - TM + (n - 3 * (32 - TM)) % (TM ? TM : 1); }
constexpr int g1_t(int n) { return n < 3 * (32 - TM) ? n % 3 : (n - 3 * (32 - TM)) / (TM ? TM : 1); }
constexpr int g2_i(int n) { return n < 2 * (22 - TM2) ? n / 2 : 22 - TM2 + (n - 2 * (22 - TM2)) % (TM2 ? TM2 : 1); }
constexpr int g2_t(int n) { return n < 2 * (22 - TM2) ? n % 2 : (n - 2 * (22 - TM2)) / (TM2 ? TM2 : 1); }
static_assert((TM2 == 0 || TM2 == 4) && W2 > TM2, "");
constexpr int g1_cnt(int n) { int c = V1; for (int k = 0; k < n; ++k) if (c < 32 && c < g1_i(k) + W1 && g1_t(k) != 1) ++c; return c; }
constexpr int g2_cnt(int n) { int c = V2; for (int k = 0; k < n; ++k) if (c < 22 && c < g2_i(k) + W2) ++c; return c; }
static_assert(TM % 2 == 0 && TM < W1, "");
constexpr int count_of(const char* s, int n, char c) { int k = 0; for (int i = 0; i < n; ++i) k += s[i] == c; return k; }
constexpr int count_before(const char* s, int n) { int k = 0; for (int i = 0; i < n; ++i) k += s[i] == s[n]; return k; }
DEV unsigned next_seed(unsigned sd) { return (((sd | ~TAGM) + 1u) & TAGM) | ((sd + 8u) & ~TAGM); }
struct Sync { unsigned base, mine, one; };
DEV void sig(const Sync& y, int slot) { FENCE(); *lp<volatile unsigned char>(y.mine + 8 * slot) = (unsigned char)y.one; FENCE(); }
DEV u64 flagrd(const Sync& y, int slot) { FENCE(); const u64 f = *lp<volatile u64>(y.base + 8 * slot); FENCE(); return f; }
DEV bool flagok(u64 f) { FENCE(); return __builtin_amdgcn_ballot_w64(f != 0x0101010101010101ull) == 0; }
DEV void waitall(const Sync& y, int slot) { while (!flagok(flagrd(y, slot))) {} }
#define WAIT_FILL(slot, fill, reads, pins) { const u64 f0_ = flagrd(Y, slot); reads; FENCE(); fill; FENCE(); pins; if (!flagok(f0_)) WAIT_READ(slot, reads; pins); }
#define WAIT_READ(slot, ...) for (;;) { const u64 f_ = flagrd(Y, slot); __VA_ARGS__; if (flagok(f_)) break; }
DEV void setprog(const Sync& y, unsigned e) { FENCE(); *lp<volatile unsigned>(y.mine) = e; FENCE(); }
DEV unsigned progrd(const Sync& y) { FENCE(); const u2 f = *lp<volatile u2>(y.base + F_PROG); FENCE(); return min(f.x, f.y); }
DEV void waitprog(const Sync& y, unsigned e) { while (__builtin_amdgcn_ballot_w64(progrd(y) < e)) {} }
DEV void waitall_slow(const Sync& y, int slot) { while (!flagok(flagrd(y, slot))) __builtin_amdgcn_s_sleep(1); }
template <int AUX> DEV void dma16(rsrc_t r, unsigned dst, unsigned v, unsigned s) { __builtin_amdgcn_raw_ptr_buffer_load_lds(r, lp<void>(dst), 16, v, s, 0, AUX); }
#define WSUF ""
DEV u4 rawrsrc(u64 p, unsigned bytes) { u4 r = {(unsigned)p, (unsigned)(p >> 32) & 0xFFFFu, bytes, 0x00020000u}; return r; }
#define DMA_ASM(op, suf, r, dst, v, s) asm volatile("s_mov_b32 m0, %2\n\ts_nop 0\n\t" op " %0, %1, %3 offen " suf "lds" ::"v"(v), "s"(r), "s"(dst), "s"(s) : "memory")
#define STAMP(i)

#define CNT(i)
__global__ void __launch_bounds__(640, 3)
fused_kernel(const unsigned short* __restrict__ x, unsigned char* ws, const unsigned short* __restrict__ w_r, const unsigned short* __restrict__ w_i,
             const unsigned short* __restrict__ w1, const unsigned short* __restrict__ w2, const unsigned short* __restrict__ b1,
             const unsigned short* __restrict__ b2, unsigned short* __restrict__ out) {
  {
    u64 pc; asm volatile("s_getpc_b64 %0" : "=s"(pc));
    const unsigned sl = ((blockIdx.x >> 3) & (32768 / CSL - 1)) * CSL;
    DMA_ASM("buffer_load_dword", "", rawrsrc(pc + sl, sl < CODE_BYTES ? CSL : 0), L_SINK, threadIdx.x * 64, 0u);
  }
  const int tid = threadIdx.x, lane = tid & 63, u = __builtin_amdgcn_readfirstlane(tid >> 6);
  const int xcd = blockIdx.x & 7, i32 = blockIdx.x >> 3, b = xcd * 32 + i32;
  const int s0 = (b & 0xC0) | ((b & 3) << 4) | ((b >> 2) & 15);
  const int bf_ = b ^ CBX, cb = b < 64 ? bf_ : (bf_ & 0xCF) | ((bf_ & 16) << 1) | ((bf_ & 32) >> 1);
  unsigned xs = (s0 + 256 * u) * 1024, l16 = lane * 16;
  const int g = lane >> 4, c = lane & 15, q = (lane >> 2) & 3, p4 = lane & 3;
  const int ut = u ^ 2, w = ut >> 1, t = ut & 1;
  Sync Y = {L_FLAG, L_FLAG + u, 1u};
  u4 S4 = {SENT, SENT, SENT, SENT};
  unsigned cv = l16 + (lane & 32) * (w ? 16 : 0), cs = (u < 8 && !(ABL_CLR & 1)) ? WS_X2 + cb * 8192 + w * 2048 + (w ? (t ^ ((CBX >> 4) & 1)) << 9 : t << 10) : OOB;
  float cf = __builtin_amdgcn_cosf((float)(u * ((0x33221140u >> ((4 * lane) & 31)) & 7) + ((0x20202000u >> ((4 * lane) & 31)) & 3)) * 0.125f);
  unsigned aXW = l16 ^ (L_A + u * 1024 + ((u & 3) << 6));
  unsigned aCW = L_CO + min(c, 8) * 16 + 2 * u, aCR = q == g ? L_CO + p4 * 16 : L_ZERO;
  unsigned aXR = (L_A + 128 * u + q * 1024 + 32 * g + 16 * (p4 & 1) + 8 * (p4 >> 1)) ^ (q << 6);
  unsigned aEW1 = L_A + (q & 1) * 1024 + 128 * u + (4 * (q >> 1) + p4) * 16 + 4 * g;
  unsigned aEW2 = aEW1 ^ 128;
  unsigned aER1 = ((c >> 3) * 1024 + 128 * g + (c & 7) * 16) ^ (L_A + 2048 * w + ((w & 1) << 7));
  unsigned aST1 = c * 64 + g * 16;
  const int t16o = w ? 16 * t : 0;
  const float k0f = (float)(4 * g);
  f4 kf = {k0f, k0f + 1.f, k0f + 2.f, k0f + 3.f};
  f4 th1 = kf * ((float)(c + t16o) * (1.f / 32.f)), c32, s32;
  if (SHADOW >= 1) { *lp<volatile unsigned>(L_FLAG + 4 * lane) = 0u; PIN(S4); PIN(cv); asm volatile("" : "+s"(cs)); PIN(Y.base); PIN(Y.mine); PIN(Y.one); }
  if (SHADOW >= 2) { PIN(aXW); PIN(aCW); PIN(aCR); PIN(aXR); PIN(aEW1); PIN(aEW2); PIN(aER1); PIN(aST1); }
  if (SHADOW >= 3) { PIN(cf); PIN(kf); PIN(th1); }
  if (SHADOW >= 4) { c32 = cos4(th1); s32 = sin4(th1); PIN(c32); PIN(s32); }
  asm volatile("" : "+s"(xs), "+v"(l16) : "s"(x), "s"(w_r), "s"(w_i), "s"(w1), "s"(b1), "s"(w2), "s"(b2), "s"(ws) : "memory");
  const rsrc_t rx = mkrsrc(x, 2048 * 1024);
  u4 xa = __builtin_amdgcn_raw_buffer_load_b128(rx, l16, xs, NT);
  FENCE();
  __builtin_amdgcn_sched_barrier(0);
  STAMP(0);
  const rsrc_t rws = mkrsrc(ws, WS_END);
  if (SHADOW < 1) { *lp<volatile unsigned>(L_FLAG + 4 * lane) = 0u; PIN(S4); }
  stx(S4, rws, cv, cs);
  u2 seed = __builtin_amdgcn_raw_buffer_load_b64(rws, 0, WS_SEED, SC1);
  FENCE();
  if (CLRB == 5) stx(S4, rws, l16, (b < 172 && u < 8) ? WS_G + b * 8192 + u * 1024 : OOB);
  asm volatile("s_waitcnt lgkmcnt(0)\n\ts_barrier" ::: "memory");
  unsigned vo[3];
  if (u >= 8) {
    __builtin_amdgcn_s_setprio(3);
    const int jw = ((0x53627140u >> (4 * xcd)) & 7) + 64 * i32 + 8 * g;
    const unsigned vw = jw * 514 + min(64 * c, 510);
    const u4 rwp = rawrsrc((u64)(u == 9 ? w_i : w_r), ABL_W);
    DMA_ASM("buffer_load_dword", WSUF, rwp, L_SINK, vw, 0u);
    DMA_ASM("buffer_load_dword", WSUF, rwp, L_SINK, vw, 32u * 514u);
    const int ft_ = 8 * (xcd & 1) + (i32 >> 2);
    const unsigned s1_ = ft_ < 15 ? (64 * (xcd >> 1) + 4 * ((i32 + R1M * (i32 >> 2)) & 3)) * 2752 + min(192 * ft_, 2560) : OOB;
    const u4 r1p = rawrsrc((u64)w1, ABL_W1);
#pragma unroll
    for (int n = 0; n < 3; ++n) {
      const unsigned pc = 64 * n + lane, rho = (pc * 171) >> 11;
      vo[n] = (32 * (u - 8) + 16 * (rho >> 3) + 256 * (rho & 7)) * 2752 + (pc - 12 * rho) * 16;
      if (ZDUP && ft_ == 14 && pc - 12 * rho < 8) vo[n] = OOB;
    }
    if ((cb & 63) == 16 && cb >= 64) {
#pragma unroll
      for (int i = 0; i < 4; ++i) DMA_ASM("buffer_load_ushort", "", rwp, L_W1 + (u - 8) * 1024 + i * 256, lane * (8 * 514), ((cb >> 6) + 512 * i) * 514 + 512);
    } else {
#pragma unroll
      for (int i = 0; i < E1; ++i)
#pragma unroll
        for (int n = 0; n < 3; ++n) DMA_ASM("buffer_load_dwordx4", "", r1p, L_W1 + i * 6144 + n * 1024 + (u - 8) * 3072, vo[n], s1_ + i * 2752);
    }
  }
#define UNIT_DEFS() \
  int bx = blockIdx.x; asm volatile("" : "+s"(bx)); \
  const int j4 = (bx >> 3) & 3, kq = (bx >> 1) & 3, ft = 8 * (bx & 1) + (bx >> 5), f0 = min(96 * ft, 1280), rot = (j4 + R1M * (bx >> 5)) & 3; \
  const bool g1 = ft < 15; \
  const int hd = XD1 ? (bx >> 5) & 1 : bx & 1, dt = 4 * hd + j4, st = XD1 ? 4 * (bx & 7) + (bx >> 6) : 8 * kq + (bx >> 5), rot2 = j4; \
  int cbx = cb; asm volatile("" : "+s"(cbx)); \
  const int pw = cbx >> 6, pt = (cbx >> 5) & 1, pp2 = pw ? pw : 4 * pt, k2b = (cbx >> 1) & 15; \
  const bool mir = k2b >= 8, sym = (pw != 0) & ((cbx & 63) == 16), dc = (pw != 0) & ((cbx & 63) == 0), spec = ((cbx & 0xC1) == 0) & ((k2b == 0) | (k2b > 8))
  int u8 = u; asm volatile("" : "+s"(u8));
  if (u8 >= 8) {
    UNIT_DEFS();
    PIN(Y.base);
    const int lw = u - 8, kb = 8 * (cbx & 1) + 32 * k2b + (pw ? 16 * pt : 0);
    const rsrc_t rw = mkrsrc(lw == 1 ? w_i : w_r, ABL_W);
    Y.mine = L_FLAG + F_PROG + 4 * lw;
    const rsrc_t r1 = mkrsrc(w1, ABL_W1), r2 = mkrsrc(w2, ABL_W2);
    unsigned vm[8], vo2[2];
    const unsigned s1 = g1 ? 64 * kq * 2752 + f0 * 2 : OOB;
    if (lw) dma16<0>(mkrsrc(&TABS, sizeof(Tabs)), L_TAB, l16, 0);
    __builtin_amdgcn_raw_ptr_buffer_load_lds(lw ? mkrsrc(b2, 4096) : mkrsrc(b1, 2752), lp<void>(L_B1 + lw * 256), 4, lane * 4, lw ? st * 128 : f0 * 2, 0, 0);
    {
      const unsigned co = (lane & 32) ? (spec ? (mir ? 528 - kb : 16) * 2 : OOB) : (mir ? 505 - kb : kb) * 2;
#pragma unroll
      for (int i = 0; i < 8; ++i) { const int j = pp2 + 8 * (32 * i + (lane & 31)); vm[i] = (mir ? (2048 - j) & 2047 : j) * 514 + co; PIN(vm[i]); }
    }
#pragma unroll
    for (int n = 0; n < 2; ++n) { const int rho = 8 * (2 * lw + n) + (lane >> 3); vo2[n] = rho * 4096 + st * 128 + (((lane & 7) ^ (((rho >> 1) & 1) << 2)) * 16); PIN(vo2[n]); }
    if (sym) VMWAIT(1); else VMWAIT(3 * E1 + 1);
    STAMP(16);
    static_for<0, (int)sizeof(SCHED) - 1>([&](auto n_) __attribute__((always_inline)) {
      constexpr char ch = SCHED[decltype(n_)::v];
      constexpr int k = count_before(SCHED, decltype(n_)::v);
      if constexpr (ch >= 'A' && ch <= 'Z') { if (LSPIN) waitall(Y, ch - 'A'); else waitall_slow(Y, ch - 'A'); }
      if constexpr (ch == '<') __builtin_amdgcn_s_setprio(0);
      if constexpr (ch == '>') __builtin_amdgcn_s_setprio(3);
      if constexpr (ch == 'w') VMWAIT(24);
      if constexpr (ch == '.') __builtin_amdgcn_s_sleep(4);
      if constexpr (ch == '_') __builtin_amdgcn_s_sleep(12);
      if constexpr (ch == '!') { VMWAIT(0); setprog(Y, 20); STAMP(18); }
      if constexpr (ch == '@') { VMWAIT(0); setprog(Y, 23); STAMP(19); }
      if constexpr (ch == '$') { VMWAIT(0); setprog(Y, 29); }
      if constexpr (ch == '%') { VMWAIT(16); setprog(Y, 29); }
      if constexpr (ch == '(') { VMWAIT(0); setprog(Y, 10); }
      if constexpr (ch == '[') { VMWAIT(4); setprog(Y, 10); }
      if constexpr (ch == ')') { VMWAIT(8); setprog(Y, 20); STAMP(18); }
      if constexpr (ch == 'm') dma16<0>(rw, L_WF + lw * 8192 + k * 1024, vm[k & 7], 0);
      if constexpr (ch == '+') { VMWAIT(count_of(SCHED, decltype(n_)::v, 'm')); setprog(Y, 1); }
      if constexpr (ch == 'g') {
#pragma unroll
        for (int i = 0; i < 3; ++i) stx(S4, rws, l16, WS_G + (b * 6 + lw * 3 + i) * 1024);
      }
      if constexpr (ch == 'h') {
#pragma unroll
        for (int i = 0; i < 4; ++i) stx(S4, rws, l16, WS_HT + (b * 8 + lw * 4 + i) * 1024);
      }
      if constexpr (ch == 'n') { VMWAIT(0); setprog(Y, 2); STAMP(17); }
      if constexpr ((ch >= '0' && ch <= '9') || (ch >= 'a' && ch <= 'f')) {
        constexpr int sl = ch <= '9' ? ch - '0' : ch - 'a' + 10;
#pragma unroll
        for (int n = 0; n < 3; ++n) dma16<0>(r1, L_W1 + sl * 6144 + (3 * lw + n) * 1024, vo[n], s1 + ((sl + 4 * rot) & 15) * 2752);
      }
      if constexpr (ch == 'x' || ch == 'y') {
        const int v0 = 2 * ((ch == 'y') ^ hd);
        int ks = k + 3 * rot2; if (ks >= 11) ks -= 11;
#pragma unroll
        for (int v = 0; v < 2; ++v)
#pragma unroll
          for (int n = 0; n < 2; ++n) dma16<0>(r2, L_W2 + k * 4096 + lw * 2048 + (v0 + v) * 12288 + n * 1024, vo2[n], (11 * (v0 + v) + ks) * (32 * 4096));
      }
      if constexpr (ch >= 'o' && ch <= 'v') {
        constexpr int k = ch - 'o';
        int ks = k + 3 + 3 * rot2; if (ks >= 11) ks -= 11;
#pragma unroll
        for (int v = 0; v < 4; ++v)
#pragma unroll
          for (int n = 0; n < 2; ++n) dma16<0>(r2, (k < 6 ? k * 16384 + v * 4096 : L_W2 + (k - 6) * 4096 + v * 12288) + lw * 2048 + n * 1024, vo2[n], (11 * v + ks) * (32 * 4096));
      }
      if constexpr (ch == '*') if (lw == 0) {
        PIN(seed);
        const unsigned sd = __builtin_amdgcn_readfirstlane(seed.x), NS = next_seed(sd);
#pragma unroll 1
        for (unsigned s = 0; s < SPIN_CAP; ++s) {
          FENCE();
          const u2 fl = __builtin_amdgcn_raw_buffer_load_b64(rws, lane * 128, WS_HF + 64 * (st & 3) * 128 + (dt >> 1) * 8, SC1);
          if (!__builtin_amdgcn_ballot_w64(((fl.x ^ NS) | (fl.y ^ ~NS)) != 0)) break;
        }
      }
    });
    VMWAIT(0);
    setprog(Y, 31);
    return;
  }

  if (CLRB == 4) stx(S4, rws, l16, b < 172 ? WS_G + b * 8192 + u * 1024 : OOB);
  if (CLRB == 6) stx(S4, rws, l16, u < 6 ? WS_G + (b * 6 + u) * 1024 : OOB);
  const float tp = (float)(s0 * (w ? w : 4 * t)) * (1.f / 2048.f);
  const f4 Z = {0.f, 0.f, 0.f, 0.f};
  f4 c16, s16;
  u4 FR, FI, xr;
  unsigned T, sd, t0;

  {
    const f4 th = th1;
    if (SHADOW < 4) { c32 = cos4(th); s32 = sin4(th); }
    u4 Mr0 = ziph(c32, s32), Mi0 = ziphn(s32, c32), GR, GI;
    asm volatile("" : "=v"(GR), "=v"(GI));
    if (w == 0) {
      const u4 alt = {0x3C00u, 0xBC00u, 0x3C00u, 0xBC00u};
      Mr0 = (Mr0 & 0xFFFFu) << (16 * t); Mi0 = (c ? (Mi0 & 0xFFFFu) : alt) << (16 * t);
      const int mm = c == 0 ? 1 : (c <= 8 ? 2 * c : 2 * c - 1);
      const f4 ps = kf * ((float)mm * (1.f / 32.f)) + tp;
      const u4 gg = ziph(cos4(ps), -sin4(ps));
      GR = (gg & 0xFFFFu) << (16 * (mm & 1)); GI = (gg >> 16) << (16 * (mm & 1));
    }
    const unsigned sg = (c & 1) ? 0x80008000u : 0u;
    u4 Mr1 = Mr0 ^ sg, Mi1 = Mi0 ^ sg;
    f4 ang = th * (1.f / 16.f) + ((w == 0 && c == 0) ? 0.f : tp);
    f4 tc, ts;
    { tc = cos4(ang); ts = sin4(ang); PIN(tc); PIN(ts); }
    { c16 = c32 * c32 - s32 * s32; s16 = (s32 + s32) * c32; FR = ziph(c16, s16); FI = ziphn(s16, c16); PIN(FR); PIN(FI); }
    unsigned aEW = aEW1, aER = aER1, aST = aST1;
    PIN(Mr0); PIN(Mi0); PIN(Mr1); PIN(Mi1); PIN(GR); PIN(GI); PIN(cf);
    PIN(aXW); PIN(aCW); PIN(aCR); PIN(aXR); PIN(aEW); PIN(aEW2); PIN(aER); PIN(aST); PIN(Y.base); PIN(Y.mine); PIN(Y.one);
    unsigned sX1 = WS_X1 + b * 8192 + ut * 1024; PINS(sX1);
    u64 selm = __builtin_amdgcn_ballot_w64(w == 0 && g == 0), cfm = __builtin_amdgcn_ballot_w64(c < 8); PINS(selm); PINS(cfm);
    float e256 = 256e-5f; PIN(e256);
    STAMP(13);
    PIN(xa);
    STAMP(1);
    *lp<u4>(aXW) = xa;
    FENCE();
    const f2 sq = swap32(dot2(xa[3], 0x3F803F80u, dot2(xa[2], 0x3F803F80u, dot2(xa[1], 0x3F803F80u, dot2(xa[0], 0x3F803F80u, 0.f)))),
                         dot2(xa[3], xa[3], dot2(xa[2], xa[2], dot2(xa[1], xa[1], dot2(xa[0], xa[0], 0.f)))));
    float tt = sq.x + sq.y;
    const f2 t16 = swap16(tt, tt);
    tt = row16_sum(t16.x + t16.y) * (1.f / 512.f);
    const f2 tot = swap32(tt, tt);
    const float mean = tot.x, rs = __builtin_amdgcn_rsqf(fmaf(fmaxf(fmaf(-mean, mean, tot.y), 0.f), 256.f, e256));
    *lp<unsigned short>(aCW) = (unsigned short)pkb(__builtin_amdgcn_inverse_ballot_w64(cfm) ? cf * rs : -mean, 0.f);
    sig(Y, 1);
    u4 A0, A1, Mn;
    WAIT_READ(1, xr = cat(trrd(aXR), trrd(aXR + 4096)); A0 = *lp<u4>(aCR); A1 = *lp<u4>(aCR + 64); Mn = *lp<u4>(Y.base + (L_CO + 128 - L_FLAG)); PIN4(xr, A0, A1, Mn));
    STAMP(22);
    const f4 d0 = mfma_b(A0, xr, mfma_b(A0, Mn, Z)), d1 = mfma_b(A1, xr, mfma_b(A1, Mn, Z));
    *lp<unsigned>(aEW) = pkh(d0[0], d0[1]);
    *lp<unsigned>(aEW2 + 2048) = pkh(d0[2], d0[3]);
    *lp<unsigned>(aEW + 4096) = pkh(d1[0], d1[1]);
    *lp<unsigned>(aEW2 + 6144) = pkh(d1[2], d1[3]);
    sig(Y, 2);
    u4 e0, e1;
    WAIT_READ(2, e0 = *lp<u4>(aER); e1 = *lp<u4>(aER + 512); PIN2(e0, e1));
    STAMP(23);
    f4 zr = mfma_h(e1, Mr1, mfma_h(e0, Mr0, Z)), zi = mfma_h(e1, Mi1, mfma_h(e0, Mi0, Z));
    cmulc(zr, zi, tc, ts);
    const u4 zp = ziph(zr, zi);
    const f4 gr = mfma_h(zp, GR, Z), gi = mfma_h(zp, GI, Z);
    f4 yr = mfma_h(zp, FR, Z), yi = mfma_h(zp, FI, Z);
    __builtin_amdgcn_sched_barrier(0);
    PIN(seed);
    sd = __builtin_amdgcn_readfirstlane(seed.x);
    T = ((sd | ~TAGM) + 1u) & TAGM;
    const bool sel = __builtin_amdgcn_inverse_ballot_w64(selm);
    yr[0] = sel ? gr[0] : yr[0]; yi[0] = sel ? gi[0] : yi[0];
    stx((ziph(yr, yi) & ~TAGM) | T, rws, aST, sX1);
    __builtin_amdgcn_sched_barrier(0);
    if (CLRA == 2) stx(S4, rws, l16, WS_HT + b * 8192 + u * 1024);
    if (CLRB == 7) stx(S4, rws, l16, b < 172 ? WS_G + b * 8192 + u * 1024 : OOB);
    t0 = clk();
    sig(Y, 3);
    *lp<float>(L_MEAN + 4 * u) = mean;
    *lp<unsigned>(L_PART + 4 * lane) = SENT;
  }
  STAMP(2);
  asm volatile("" ::"s"(out));
  UNIT_DEFS();
  u4 IR = zipbn(c16, s16), II = zipb(s16, c16), AI00, AI01, AI10, AI11;
  u2 ba, bb; unsigned bias2;
  int lz = lane;
  {
    const f4 a2 = kf * ((float)c * (1.f / 256.f));
    f4 tc = cos4(a2), ts = sin4(a2);
    unsigned aLD = (tid >> 1) * 8192 + (tid & 1) * 16;
    unsigned aPW = L_A + (tid & 1) * 4096 + (((tid >> 1) * 4) ^ ((tid & 1) << 6));
    unsigned aPR = L_A + u * 1024 + ((16 * lane) ^ ((u >> 2) << 6));
    unsigned aST = tid * 16;
    PIN(tc); PIN(ts); PIN(IR); PIN(II); PIN(aLD); PIN(aPW); PIN(aPR); PIN(aST);
    const int wc = ((spec | dc) && u == 0) ? 256 : (mir ? 7 - u : u);
    unsigned aW = L_WF + c * 16 + g * 2048 + (wc >> 2) * 8; PIN(aW);
    const unsigned wsel = 0x01000C0Cu + (wc & 3) * 0x02020000u;
    waitprog(Y, 1);
    {
      const unsigned ta = L_TAB + (q == g ? p4 : 4) * 64, fa = L_B1 + (u >= 4 ? (u - 4) * 32 + (lane >> 5) * 8 : 128 + (u >> 1) * 32 + (u & 1) * 8 + (lane & 1) * 16);
      AI00 = *lp<u4>(ta); AI01 = *lp<u4>(ta + 16); AI10 = *lp<u4>(ta + 32); AI11 = *lp<u4>(ta + 48);
      ba = *lp<u2>(fa); bb = *lp<u2>(fa + 16); bias2 = *lp<unsigned short>(L_B2 + (FULL ? u * 16 + (lane >> 3) * 2 : (w >> 1) * 64 + (lane & 31) * 2));
    }
    waitall(Y, 3);
    if (CLRA == 1) stx(S4, rws, aST, (ABL_CLR & 2) ? OOB : WS_HT + b * 8192);
    pause(t0, PRE1);
    STAMP(14);
    FENCE();
    u4 v = ldx(rws, aLD, WS_X1 + cb * 32);
    FENCE();
    sig(Y, 15);
    if (CLRB == 3) stx(S4, rws, aST, b < 172 ? WS_G + b * 8192 : OOB);
    if (!CLRA) stx(S4, rws, aST, (ABL_CLR & 2) ? OOB : WS_HT + b * 8192);
    f4 wre, wim;
#define W_READ() do { \
    waitprog(Y, 2); \
    _Pragma("unroll") for (int i = 0; i < 4; ++i) { \
      const u2 a = *lp<u2>(aW + (i & 1) * 256 + (i >> 1) * 1024), bq = *lp<u2>(aW + (i & 1) * 256 + (i >> 1) * 1024 + 8192); \
      wre[i] = __builtin_bit_cast(float, __builtin_amdgcn_perm(a.y, a.x, wsel)); wim[i] = __builtin_bit_cast(float, __builtin_amdgcn_perm(bq.y, bq.x, wsel) ^ (mir ? 0x80000000u : 0u)); } \
    if (sym && u == 0) { \
      _Pragma("unroll") for (int i = 0; i < 4; ++i) { wre[i] = 0.5f * (wre[i] + blo(*lp<unsigned short>(L_W1 + c * 4 + g * 256 + i * 64))); wim[i] = 0.5f * (wim[i] + blo(*lp<unsigned short>(L_W1 + 1024 + c * 4 + g * 256 + i * 64))); } \
    } \
    PIN(wre); PIN(wim); } while (0)
    if (!WLATE) W_READ();
    PIN(v);
    if (__builtin_expect(anyuntagged(tagdiff(0u, v, T)), 0)) {
#pragma unroll 1
      for (unsigned s = 0; s < SPIN_CAP; ++s) { FENCE(); CNT(0); v = ldx(rws, aLD, WS_X1 + cb * 32); if (!anyuntagged(tagdiff(0u, v, T))) break; }
    }
    STAMP(3);
    v ^= (T ^ TAGMID);
#pragma unroll
    for (int i = 0; i < 4; ++i) *lp<unsigned>(aPW + i * 1024) = v[i];
    sig(Y, 4);
    u4 a;
    WAIT_READ(4, a = *lp<u4>(aPR); PIN(a));
    f4 r = mfma_h(a, FR, Z), im = mfma_h(a, FI, Z);
    cmulc(r, im, tc, ts);
    u4 z = ziph(r, im);
    if (WLATE) { PIN(z); W_READ(); }
    r = mfma_h(FR, z, Z); im = mfma_h(FI, z, Z);
    cmul(r, im, wre, wim);
    z = zipb(r, im);
    r = mfma_b(z, IR, Z); im = mfma_b(z, II, Z);
    cmul(r, im, tc, ts);
    z = zipb(r, im);
    r = mfma_b(IR, z, Z); im = mfma_b(II, z, Z);
    *lp<u4>(aPR) = zipb(r, im);
    VMWAIT(0);
    sig(Y, 5);
    u4 ov;
    WAIT_READ(5, ov[0] = *lp<unsigned>(aPW); ov[1] = *lp<unsigned>(aPW + 1024); ov[2] = *lp<unsigned>(aPW + 2048); ov[3] = *lp<unsigned>(aPW + 3072); PIN(ov));
    stx(ov, rws, aST, WS_X2 + cb * 8192);
    if (CLRB == 2) stx(S4, rws, aST, b < 172 ? WS_G + b * 8192 : OOB);
    t0 = clk();
    sig(Y, 6);
  }
  STAMP(4);
  {
    f4 th = (kf + (float)t16o) * ((float)c * (1.f / 32.f));
    f4 ang = th * (1.f / 16.f) + tp;
    if (w == 0 && g == 0) ang[0] = 0.f;
    f4 tc = cos4(ang), ts = sin4(ang);
    const u4 ZU = {};
    u4 GR, GI;
    asm volatile("" : "=v"(GR), "=v"(GI));
    if (w == 0) {
      f4 mm = 2.f * kf - (g >= 2 ? 1.f : 0.f); if (g == 0) mm[0] = 1.f; if (g == 2) mm[0] = 16.f;
      const f4 ps = mm * ((float)c * (1.f / 32.f)) + tp; f4 gc = cos4(ps), gs = -sin4(ps);
      if (g == 2) { gc[0] *= 0.5f; gs[0] *= 0.5f; }
      const u4 gg = zipb(gc, gs);
      GR = g < 2 ? gg : ZU; GI = g < 2 ? ZU : gg;
      if (!(g & 1)) { const unsigned sw = GR[0]; GR[0] = GI[0]; GI[0] = sw; }
    }
    unsigned aLD = (8 * g + (c >> 3)) * 8192 + (c & 7) * 4;
    const int re_ = 2 * ut, ro_ = ut ? 2 * ut + 1 : 2;
    const unsigned wo = 32 * c + 8 * (g ^ q);
    unsigned aEWe = wo ^ (L_E + re_ * 1024 + ((re_ & 3) << 6)), aEWo = wo ^ (L_E + ro_ * 1024 + ((ro_ & 3) << 6));
    unsigned aER = (L_E + 128 * u + q * 1024 + 32 * g + 16 * (p4 & 1) + 8 * (p4 >> 1)) ^ (q << 6) ^ (8 * (u & 3)), aER0 = q == 1 ? aER + 1024 : aER;
    unsigned aPW = L_PART + (lane >> 3) * 32 + u * 4, aPR = L_PART + (lane & 7) * 32 + (lane & 8) * 2, sHT = WS_HT + u * 262144 + b * 1024;
    float eps2 = 1e-10f; PIN(eps2); asm volatile("" : "+s"(sHT));
    PIN(tc); PIN(ts); PIN(GR); PIN(GI); PIN(aLD); PIN(aEWe); PIN(aEWo); PIN(aER); PIN(aER0); PIN(aPW); PIN(aPR);
    pause(t0, PRE2);
    STAMP(15);
    u4 z;
    FENCE();
#pragma unroll
    for (int i = 0; i < 4; ++i) z[i] = ldw(rws, aLD, WS_X2 + b * 32 + (ut * 32 + 2 * i) * 8192);
    FENCE();
    sig(Y, 17);
    if (!CLRB) stx(S4, rws, l16, (b < 172 && !(ABL_CLR & 4)) ? WS_G + b * 8192 + u * 1024 : OOB);
    PIN(th);
    const f4 m0 = *lp<f4>(L_MEAN), m1 = *lp<f4>(L_MEAN + 16);
    const f4 c32 = cos4(th), s32 = sin4(th);
    u4 Me0 = zipbn(c32, s32), Mo0 = zipb(s32, c32);
    if (w == 0) {
      if (g == 0) Me0[0] = (c & 1) ? 0xBF803F80u : 0x3F803F80u;
      Mo0 = t ? Me0 : ZU; Me0 = t ? ZU : Me0;
    }
    const u4 SG = {0u, 0x80008000u, 0u, 0x80008000u};
    u4 Me1 = Me0 ^ SG, Mo1 = Mo0 ^ SG;
    f4 xc0 = unb(xr[0], xr[1]) - m0, xc1 = unb(xr[2], xr[3]) - m1;
    PIN(xc0); PIN(xc1); PIN(Me0); PIN(Mo0); PIN(Me1); PIN(Mo1);
    waitall(Y, 6);
    PIN(z);
    if (__builtin_expect(anysent(mx(0u, z)), 0)) {
#pragma unroll 1
      for (unsigned s = 0; s < SPIN_CAP; ++s) {
        FENCE(); CNT(1);
#pragma unroll
        for (int i = 0; i < 4; ++i) z[i] = ldw(rws, aLD, WS_X2 + b * 32 + (ut * 32 + 2 * i) * 8192);
        if (!anysent(mx(0u, z))) break;
      }
    }
    STAMP(5);
    sig(Y, 19);
    const f4 gr = mfma_b(z, GR, Z), gi = mfma_b(z, GI, Z);
    f4 r = mfma_b(z, IR, Z), im = mfma_b(z, II, Z);
    const bool sel = w == 0 && g == 0;
    r[0] = sel ? gr[0] : r[0]; im[0] = sel ? gi[0] : im[0];
    cmul(r, im, tc, ts);
    const u4 U = zipb(r, im);
    const f4 e0 = mfma_b(U, Me0, Z), o0 = mfma_b(U, Mo0, Z), e1 = mfma_b(U, Me1, Z), o1 = mfma_b(U, Mo1, Z);
    __builtin_amdgcn_sched_barrier(0);
    const u4 pe = catb(e0, e1), po = catb(o0, o1);
    *lp<u2>(aEWe) = pe.xy; *lp<u2>(aEWo) = po.xy;
    *lp<u2>(aEWe + 512) = pe.zw; *lp<u2>(aEWo + 512) = po.zw;
    sig(Y, 7);
    u4 k0, k1;
    WAIT_READ(7, k0 = cat(trrd(aER0), trrd(aER + 4096)); k1 = cat(trrd(aER + 8192), trrd(aER + 12288)); PIN2(k0, k1));
    f4 h0 = mfma_b(AI01, k1, mfma_b(AI00, k0, xc0)), h1 = mfma_b(AI11, k1, mfma_b(AI10, k0, xc1));
    const f4 q0 = h0 * h0, q1 = h1 * h1;
    const f2 a0 = swap32(q0[0], q1[0]), a1 = swap32(q0[1], q1[1]), a2 = swap32(q0[2], q1[2]), a3 = swap32(q0[3], q1[3]);
    const f2 b0 = swap16(a0.x + a0.y, a2.x + a2.y), b1_ = swap16(a1.x + a1.y, a3.x + a3.y);
    float pr;
    asm volatile("s_nop 1\n\tv_add_f32_dpp %0, %1, %1 row_ror:8 row_mask:0xf bank_mask:0x3\n\tv_add_f32_dpp %0, %2, %2 row_ror:8 row_mask:0xf bank_mask:0xc\n\t"
                 "s_nop 1\n\tv_add_f32_dpp %0, %0, %0 row_half_mirror row_mask:0xf bank_mask:0xf\n\t"
                 "s_nop 1\n\tv_add_f32_dpp %0, %0, %0 quad_perm:[2,3,0,1] row_mask:0xf bank_mask:0xf\n\t"
                 "s_nop 1\n\tv_add_f32_dpp %0, %0, %0 quad_perm:[1,0,3,2] row_mask:0xf bank_mask:0xf\n\tv_min_u32 %0, -2, %0"
                 : "=&v"(pr) : "v"(b0.x + b0.y), "v"(b1_.x + b1_.y));
    FENCE();
    *lp<float>(aPW) = pr;
    u4 pq;
    do { FENCE(); pq = *lp<u4>(aPR); } while (anysent(mx(0u, pq)));
    FENCE();
    const f4 pf = __builtin_bit_cast(f4, pq);
    const float rsv = __builtin_amdgcn_rsqf(fmaf(ror_add<0x128>((pf[0] + pf[2]) + (pf[1] + pf[3])), (1.f + 1e-5f) / 512.f, eps2));
    f4 n0, n1;
    asm volatile("s_nop 1\n\t"
                 "v_mul_f32_dpp %0, %8, %9 row_newbcast:0 row_mask:0xf bank_mask:0xf\n\tv_mul_f32_dpp %1, %8, %10 row_newbcast:1 row_mask:0xf bank_mask:0xf\n\t"
                 "v_mul_f32_dpp %2, %8, %11 row_newbcast:2 row_mask:0xf bank_mask:0xf\n\tv_mul_f32_dpp %3, %8, %12 row_newbcast:3 row_mask:0xf bank_mask:0xf\n\t"
                 "v_mul_f32_dpp %4, %8, %13 row_newbcast:4 row_mask:0xf bank_mask:0xf\n\tv_mul_f32_dpp %5, %8, %14 row_newbcast:5 row_mask:0xf bank_mask:0xf\n\t"
                 "v_mul_f32_dpp %6, %8, %15 row_newbcast:6 row_mask:0xf bank_mask:0xf\n\tv_mul_f32_dpp %7, %8, %16 row_newbcast:7 row_mask:0xf bank_mask:0xf"
                 : "=&v"(n0[0]), "=&v"(n0[1]), "=&v"(n0[2]), "=&v"(n0[3]), "=&v"(n1[0]), "=&v"(n1[1]), "=&v"(n1[2]), "=&v"(n1[3])
                 : "v"(rsv), "v"(h0[0]), "v"(h0[1]), "v"(h0[2]), "v"(h0[3]), "v"(h1[0]), "v"(h1[1]), "v"(h1[2]), "v"(h1[3]));
    stx(catb(n0, n1), rws, l16, sHT);
    __builtin_amdgcn_sched_barrier(0);
    PIN(h0); PIN(h1);
    t0 = clk();
    STAMP(6);
    FENCE();
    PIN(lz);
    const int dn = (lz & 0x33) | ((lz & 4) << 1) | ((lz & 8) >> 1);
    *lp<u4>(L_A + u * 1024 + dn * 16) = catb(h0 + m0, h1 + m1);
    sig(Y, 18);
  }
  const unsigned NS = next_seed(sd);
#define H_FLAG() do { VMWAIT(0); const u2 fl = {NS, ~NS}; if (lane == 0) __builtin_amdgcn_raw_buffer_store_b64(fl, rws, 0, WS_HF + b * 128 + (u - 4) * 8, SC1); } while (0)
  if (u >= 4) {
    waitall_slow(Y, 18);
    const unsigned ph = L_A + (u - 4) * 2048 + (16 * g + 8 * (p4 >> 1) + q) * 16 + 8 * (p4 & 1);
    const u4 ha = cat(trrd(ph), trrd(ph + 64)), hb = cat(trrd(ph + 1024), trrd(ph + 1088));
    sig(Y, 8); sig(Y, 21);
    if (PREH) pause(t0, PREH); else waitall_slow(Y, 8);
    stx(ha, rws, (c & 7) * (HLAY ? 128 : 262144) + (2 * g + (c >> 3)) * 16, HLAY ? WS_H + s0 * 8192 + (u - 4) * 2048 : WS_H + s0 * 1024 + (u - 4) * 256);
    stx(hb, rws, (c & 7) * (HLAY ? 128 : 262144) + (2 * g + (c >> 3)) * 16, HLAY ? WS_H + s0 * 8192 + (u - 4) * 2048 + 1024 : WS_H + s0 * 1024 + (u - 4) * 256 + 128);
    if (HFE) H_FLAG();
  }
#define H_FLAG_UNUSED() do { VMWAIT(0); const u2 fl = {NS, ~NS}; if (lane == 0) __builtin_amdgcn_raw_buffer_store_b64(fl, rws, 0, WS_HF + b * 128 + (u - 4) * 8, SC1); } while (0)
  u4 WB[4][2];
  unsigned bA[2];
  int ln = lane;
#define WB_PROLOGUE() do { PIN(ln); \
    _Pragma("unroll") for (int nt = 0; nt < 2; ++nt) bA[nt] = L_W2 + w * 12288 + (4 * (ln >> 5) + ((ln >> 2) & 3)) * 128 + ((nt ^ (FULL ? 0 : w >> 1) ^ ((ln >> 3) & 1)) * 64) + 2 * (ln & 16) + 8 * (ln & 3); \
    waitprog(Y, 23); _Pragma("unroll") for (int i = 0; i < 4; ++i) _Pragma("unroll") for (int nt = 0; nt < 2; ++nt) WB[i][nt] = cat(trrd(bA[nt] + i * 2048), trrd(bA[nt] + i * 2048 + 1024)); sig(Y, 12); } while (0)
  if (g1) {
    const int h = lane >> 5;
    const unsigned v32 = h * 1024 + (lane & 31) * 16;
    const unsigned pb = WS_P + (ft * 4 + j4) * (16 * 6144);
    if (u < 4) {
      const unsigned a1 = L_W1 + (8 * h + q) * 192 + 32 * (g & 1) + 8 * p4;
      unsigned a1h = a1 + 49152; PIN(a1h);
      u4 WA[6];
#pragma unroll
      for (int n = 0; n < 6; ++n) WA[n] = cat(trrd(a1 + (n / 3) * 3072 + (n % 3) * 64), trrd(a1 + (n / 3) * 3072 + (n % 3) * 64 + 768));
      const unsigned hb = WS_HT + (2 * j4 + (u >> 1)) * 262144 + kq * 65536 + (u & 1) * 512;
      unsigned hq[4];
#pragma unroll
      for (int n = 0; n < 4; ++n) { hq[n] = ABL_HT ? OOB : hb + ((n + rot) & 3) * 16384; asm volatile("" : "+s"(hq[n])); }
      pause(t0, PRE3);
      u4 B[W1];
#define LOADB(k) B[(k) % W1] = ldx(rws, v32 + ((k) & 1) * 2048, hq[(k) >> 3] + (((k) >> 1) & 3) * 4096)
      FENCE();
#pragma unroll
      for (int i = 0; i < V1; ++i) { LOADB(i); __builtin_amdgcn_sched_barrier(0); }
      sig(Y, 21);
      waitprog(Y, 10);
      unsigned pg;
      f16v acc[3] = {};
      unsigned m = MX(MX(0u, B[0]), B[1]);
      u64 bad = __builtin_amdgcn_ballot_w64(m == SENT);
      unsigned sp0 = (u == kq || ft == 14) ? OOB : pb + (u * 4 + kq) * 6144, sp2 = u == kq ? OOB : pb + (u * 4 + kq) * 6144; PINS(sp0); PINS(sp2);
#define SENDP(n) do { const f16v& a_ = acc[(n) >> 1]; constexpr int o_ = 8 * ((n) & 1); \
        const u4 tv_ = {pkh(a_[o_], a_[o_ + 1]), (pkh(a_[o_ + 2], a_[o_ + 3]) & ~TAGM) | T, pkh(a_[o_ + 4], a_[o_ + 5]), (pkh(a_[o_ + 6], a_[o_ + 7]) & ~TAGM) | T}; \
        stx(tv_, rws, l16, ((n) < 4 ? sp0 : sp2) + (n) * 1024); } while (0)
      static_for<0, 96>([&](auto n_) __attribute__((always_inline)) {
        constexpr int n = decltype(n_)::v, i = g1_i(n), nt = g1_t(n), cnt = g1_cnt(n);
        constexpr bool fresh = nt == 0;
        if constexpr (fresh && !(i & 1)) if (__builtin_expect(bad != 0, 0)) {
#pragma unroll 1
          for (unsigned s = 0; s < SPIN_CAP; ++s) { FENCE(); CNT(2); LOADB(i); LOADB(i + 1); if (!anysent(MX(MX(0u, B[i % W1]), B[(i + 1) % W1]))) break; }
#pragma unroll
          for (int k = i + 2; k < cnt; ++k) { LOADB(k); __builtin_amdgcn_sched_barrier(0); }
        }
        if constexpr (n == 0) STAMP(7);
        if constexpr (MW && n % 3 == 0) PIN(WA[(n + 2) % 6]);
        acc[nt] = mfma32(WA[n % 6], B[i % W1], acc[nt]);
        __builtin_amdgcn_sched_barrier(0);
        if constexpr (g1_cnt(n + 1) > cnt) { LOADB(cnt); __builtin_amdgcn_sched_barrier(0); }
        if constexpr (n + 6 < 96) { constexpr int i6 = g1_i(n + 6); const unsigned o = (i6 < 16 ? a1 : a1h) + (i6 & 15) * 3072 + g1_t(n + 6) * 64; WA[n % 6] = cat(trrd(o), trrd(o + 768)); }
        if constexpr (nt == (i < 32 - TM ? 1 : 0) && i + 2 < 32) {
          m = MX((i & 1) ? m : 0u, B[(i + 2) % W1]);
          if (i & 1) { bad = __builtin_amdgcn_ballot_w64(m == SENT); asm volatile("" : "+s"(bad)); } else PIN(m);
        }
        if constexpr (TM >= 4 && i >= 34 - TM && i < 36 - TM && nt >= 1) SENDP(2 * (nt - 1) + i - (34 - TM));
        if constexpr (n == 50) pg = progrd(Y);
        if constexpr (n == 53) if (__builtin_expect(__builtin_amdgcn_ballot_w64(pg < 20) != 0, 0)) waitprog(Y, 20);
        if constexpr (n == 3 * (32 - W1) + 2) sig(Y, 8);
        if constexpr (n == 89) sig(Y, 9);
        __builtin_amdgcn_sched_barrier(0);
      });
      STAMP(8);
      if (TM < 4) { SENDP(0); SENDP(1); SENDP(2); SENDP(3); }
      SENDP(4); SENDP(5);
      if (u == kq) {
        waitall(Y, 8);
#pragma unroll
        for (int n = 0; n < 12; ++n) { const f4 v = {acc[n >> 2][4 * (n & 3)], acc[n >> 2][4 * (n & 3) + 1], acc[n >> 2][4 * (n & 3) + 2], acc[n >> 2][4 * (n & 3) + 3]}; *lp<f4>(L_OWN + n * 1024 + l16) = v; }
      }
    } else sig(Y, 9);
    sig(Y, 10);
    const unsigned go = WS_G + ((2 * j4 + (kq >> 1)) * 172 + (f0 >> 3)) * 1024 + (kq & 1) * 512, pl = pb + kq * 4 * 6144, TX = T ^ TAGMID;
    if (u >= 4) {
      pause(t0, PRE3 + 3600); if (WBE) WB_PROLOGUE(); waitall_slow(Y, 9); t0 = clk() + PD4;
      waitall(Y, 10);
      if (!HFE) H_FLAG();
      f4 ua = *lp<f4>(L_OWN + (u - 4) * 2048 + l16) + unb(ba.x, ba.y), ub = *lp<f4>(L_OWN + (u - 4) * 2048 + 1024 + l16) + unb(bb.x, bb.y);
      PIN(ua); PIN(ub);
      pause(t0, PRE4);
      u4 p[3];
#pragma unroll 1
      for (unsigned s = 0; s < SPIN_CAP; ++s) {
        FENCE(); CNT(3);
#pragma unroll
        for (int n = 0; n < 3; ++n) p[n] = ldx(rws, l16, pl + (ft == 14 ? 4 : u - 4) * 1024 + ((kq + 1 + n) & 3) * 6144);
        sig(Y, 20);
        if (!anyuntagged(((p[0].y ^ T) | (p[0].w ^ T) | (p[1].y ^ T)) | ((p[1].w ^ T) | (p[2].y ^ T) | (p[2].w ^ T)))) break;
      }
      STAMP(21);
      sig(Y, 11);
      ua = addh(ua, add3h(p[0].x, p[1].x, p[2].x), add3h(p[0].y ^ TX, p[1].y ^ TX, p[2].y ^ TX));
      ub = addh(ub, add3h(p[0].z, p[1].z, p[2].z), add3h(p[0].w ^ TX, p[1].w ^ TX, p[2].w ^ TX));
      stx(catb(gelu(ua), gelu(ub)), rws, v32, ft == 14 ? OOB : go + (u - 4) * 2048);
    } else {
      t0 = clk(); if (WBE) WB_PROLOGUE();
      waitall(Y, 10);
      f4 ua = *lp<f4>(L_OWN + (8 + (u & 2) + (ln & 1)) * 1024 + (u & 1) * 512 + (ln >> 1) * 16) + unb(ba.x, ba.y);
      PIN(ua);
      pause(t0, PRE4);
      u2 p[3];
#pragma unroll 1
      for (unsigned s = 0; s < SPIN_CAP; ++s) {
        FENCE(); CNT(3);
#pragma unroll
        for (int n = 0; n < 3; ++n) p[n] = __builtin_amdgcn_raw_buffer_load_b64(rws, ln * 8, pl + 4096 + u * 512 + ((kq + 1 + n) & 3) * 6144, SC1);
        sig(Y, 20);
        if (!anyuntagged((p[0].y ^ T) | (p[1].y ^ T) | (p[2].y ^ T))) break;
      }
      STAMP(21);
      sig(Y, 11);
      const f4 gv = gelu(addh(ua, add3h(p[0].x, p[1].x, p[2].x), add3h(p[0].y ^ TX, p[1].y ^ TX, p[2].y ^ TX)));
      const u2 gp = {pkb(gv[0], gv[1]), pkb(gv[2], gv[3])};
      __builtin_amdgcn_raw_buffer_store_b64(gp, rws, ln * 8, go + (8 + u) * 1024, SC1);
    }
    t0 = clk();
  } else {
    sig(Y, 21); sig(Y, 8); sig(Y, 9); sig(Y, 10); sig(Y, 20);
    if (u >= 4 && !HFE) H_FLAG();
    if (WBE) WB_PROLOGUE();
    pause(t0, PRE3 + 2000);
#pragma unroll 1
    for (unsigned s = 0; s < SPIN_CAP; ++s) { FENCE(); if (!anysent(ldw(rws, ((u & 3) * 64 + ln) * 1024, WS_HT))) break; }
    if (!SEEDEND && b == 255 && u == 0 && ln == 0) __builtin_amdgcn_raw_buffer_store_b32(NS, rws, 0, WS_SEED, SC1);
    t0 += IDLE;
    pause(t0, 0);
    sig(Y, 11);
  }
  STAMP(9);
  {
    const int r32 = ln & 31, h = ln >> 5, srow = st * 64 + (FULL ? 8 * u + (ln >> 3) : 32 * (w >> 1) + r32);
    if (!WBE) WB_PROLOGUE();
    const unsigned dB = -w * 8192 - L_W2;
    const unsigned oo = srow * 1024 + dt * 128 + (FULL ? (ln & 7) * 16 : t * 64 + (w & 1) * 32 + h * 16);
    u4 hv;
    const unsigned gb = WS_G + dt * 172 * 1024 + t * 512, va = h * 1024 + (r32 ^ (FULL ? 0 : 16 * (w & 1))) * 16;
    unsigned gq[11];
#pragma unroll
    for (int i = 0; i < 11; ++i) { int ks = i + 3 * rot2; if (ks >= 11) ks -= 11; gq[i] = ABL_G ? OOB : gb + min(11 * w + ks, 42) * 4096; asm volatile("" : "+s"(gq[i])); }
    unsigned bL[2][2];
#pragma unroll
    for (int nt = 0; nt < 2; ++nt) { bL[0][nt] = bA[nt] + dB; bL[1][nt] = bA[nt] + dB + 49152; PIN(bL[0][nt]); PIN(bL[1][nt]); }
    unsigned aRW[2], aRR;
#pragma unroll
    for (int a = 0; a < 2; ++a) { const int ck = 4 * t + 2 * a + h; aRW[a] = w * 16384 + ck * 512 + ((r32 & 16) | ((r32 + 2 * ck) & 15)) * 16; if (FULL) PIN(aRW[a]); }
    { const int rr = 8 * (u & 3) + (ln >> 3), ck = ln & 7; aRR = ((u >> 2) * 8 + ck) * 512 + ((rr & 16) | ((rr + 2 * ck) & 15)) * 16; if (FULL) PIN(aRR); }
    waitall(Y, 11);
    pause(t0, PRE5);
    u4 A[W2];
#define LOADA(k) A[(k) % W2] = ldx(rws, va + ((k) & 1) * 2048, gq[(k) >> 1])
    FENCE();
#pragma unroll
    for (int i = 0; i < V2; ++i) { LOADA(i); __builtin_amdgcn_sched_barrier(0); }
    waitprog(Y, 29);
    unsigned pg; u64 f13;
    f16v acc[2] = {};
    unsigned m = MX(MX(0u, A[0]), A[1]);
    u64 bad = __builtin_amdgcn_ballot_w64(m == SENT);
#define PUTR(n) do { const f16v& a_ = acc[(n) >> 2]; constexpr int o_ = 4 * ((n) & 3); const f4 v_ = {a_[o_], a_[o_ + 1], a_[o_ + 2], a_[o_ + 3]}; *lp<f4>(aRW[((n) >> 1) & 1] + ((n) & 1) * 8192 + ((n) >> 2) * 4096) = v_; } while (0)
    static_for<0, 44>([&](auto n_) __attribute__((always_inline)) {
      constexpr int n = decltype(n_)::v, i = g2_i(n), nt = g2_t(n), cnt = g2_cnt(n);
      if constexpr (nt == 0 && !(i & 1)) if (__builtin_expect(bad != 0, 0)) {
#pragma unroll 1
        for (unsigned s = 0; s < SPIN_CAP; ++s) { FENCE(); CNT(4); LOADA(i); LOADA(i + 1); if (!anysent(MX(MX(0u, A[i % W2]), A[(i + 1) % W2]))) break; }
#pragma unroll
        for (int k = i + 2; k < cnt; ++k) { LOADA(k); __builtin_amdgcn_sched_barrier(0); }
      }
      if constexpr (n == 0) STAMP(10);
      if constexpr (MW && !(n & 1)) PIN(WB[(n >> 1) & 3][1]);
      acc[nt] = mfma32(A[i % W2], WB[(n >> 1) & 3][n & 1], acc[nt]);
      __builtin_amdgcn_sched_barrier(0);
      if constexpr (g2_cnt(n + 1) > cnt) { LOADA(cnt); __builtin_amdgcn_sched_barrier(0); }
      if constexpr (nt == 0 && i == (W2 <= 9 ? 23 - W2 : 14)) { hv = ldx(rws, (srow & 255) * 8192 + (oo & 127), WS_H + dt * 1024 + (st >> 2) * 128); __builtin_amdgcn_sched_barrier(0); }
      if constexpr (n + 8 < 44) {
        constexpr int i8 = g2_i(n + 8), t8 = g2_t(n + 8), k = i8 >> 1;
        const unsigned off = ((k < 3 || k >= 9) ? bA[t8] + (k % 9) * 4096 : bL[k >= 6][t8] + (k % 3) * 16384) + (i8 & 1) * 2048;
        WB[(n >> 1) & 3][n & 1] = cat(trrd(off), trrd(off + 1024));
      }
      if constexpr (nt == (i < 22 - TM2 ? 1 : 0) && i + 2 < 22) {
        m = MX((i & 1) ? m : 0u, A[(i + 2) % W2]);
        if (i & 1) { bad = __builtin_amdgcn_ballot_w64(m == SENT); asm volatile("" : "+s"(bad)); } else PIN(m);
      }
      if constexpr (n == 25) pg = progrd(Y);
      if constexpr (n == 27) { if (__builtin_expect(__builtin_amdgcn_ballot_w64(pg < 31) != 0, 0)) waitprog(Y, 31); sig(Y, 13); }
      if constexpr (n == 36) f13 = flagrd(Y, 13);
      if constexpr (n == 39) if (__builtin_expect(!flagok(f13), 0)) waitall(Y, 13);
      if constexpr (TM2 == 4 && n >= 42) { PUTR(2 * (n - 42)); PUTR(2 * (n - 42) + 1); }
      __builtin_amdgcn_sched_barrier(0);
    });
    STAMP(11);
    {
      if (TM2 != 4) { PUTR(0); PUTR(1); PUTR(2); PUTR(3); }
      PUTR(4); PUTR(5); PUTR(6); PUTR(7);
      sig(Y, 14);
      const float bias = blo(bias2);
      f4 ha = unb(hv[0], hv[1]) + bias, hb4 = unb(hv[2], hv[3]) + bias;
      PIN2(ha, hb4);
      f4 pa[4], pb[4];
      waitall(Y, 14);
#pragma unroll
      for (int v = 0; v < 4; ++v) { pa[v] = *lp<f4>(aRR + v * 16384); pb[v] = *lp<f4>(aRR + v * 16384 + 8192); }
      __builtin_amdgcn_raw_buffer_store_b128(catb(((ha + pa[0]) + pa[1]) + (pa[2] + pa[3]), ((hb4 + pb[0]) + pb[1]) + (pb[2] + pb[3])), mkrsrc(out, 2048 * 1024), oo, 0, OUTAUX);
    }
  }
  if (SEEDEND && b == 0 && u == 0 && ln == 0) __builtin_amdgcn_raw_buffer_store_b32(NS, rws, 0, WS_SEED, SC1);
}

extern "C" void kernel_launch(void* const* d_in, const int* in_sizes, int n_in, void* d_out, int out_size, void* d_ws, size_t ws_size,
                              hipStream_t stream) {
  (void)hipFuncSetAttribute((const void*)fused_kernel, hipFuncAttributeMaxDynamicSharedMemorySize, L_BYTES);
  fused_kernel<<<256, 640, L_BYTES, stream>>>((const unsigned short*)d_in[0], (unsigned char*)d_ws, (const unsigned short*)d_in[1], (const unsigned short*)d_in[2],
                                              (const unsigned short*)d_in[4], (const unsigned short*)d_in[6], (const unsigned short*)d_in[5],
                                              (const unsigned short*)d_in[7], (unsigned short*)d_out);
}
```
